```python
import torch

def _softmax(x, axis=-1):
    return torch.softmax(x, dim=axis)

def _gelu(x):
    return torch.nn.functional.gelu(x, approximate='tanh')

def _silu(x):
    return torch.nn.functional.silu(x)

def _sigmoid(x):
    return torch.sigmoid(x)

def _layernorm(x, axis=-1, eps=1e-05):
    m = x.mean(dim=axis, keepdim=True)
    v = x.var(dim=axis, keepdim=True, unbiased=False)
    return (x - m) / torch.sqrt(v + eps)

def _scatter_add(tgt, idx, src, axis=0):
    return tgt.clone().index_add_(axis, idx, src)

def _segment_sum(src, seg, n):
    out = torch.zeros((n,) + src.shape[1:], dtype=src.dtype)
    return out.index_add_(0, seg, src)

def _pad(x, pw):
    flat = tuple((v for pair in reversed(pw) for v in pair))
    return torch.nn.functional.pad(x, flat)
kernel_name = 'parallel_sum__memory_aug_recurrent_mem_tr__glu_gated_reglu__prompt'

def setup_inputs():

    _w = lambda a: (lambda t: t.to(torch.bfloat16) if t.is_floating_point() else t)(torch.as_tensor(a))

    def _A_setup():
        B, S, Dm = (1, 2048, 640)
        H, Dh = (32, 128)
        M, seg = (8, 32)
        seg = min(seg, S)
        S = S // seg * seg
        return {'x': _w((lambda *s: torch.randn(*s, dtype=torch.bfloat16) * 0.05)(B, S, Dm)), 'mem0': _w((lambda *s: torch.randn(*s, dtype=torch.bfloat16) * 0.2)(B, M, Dm)), 'Wq': _w((lambda *s: torch.randn(*s, dtype=torch.bfloat16) * 1.6)(Dm, H, Dh) * Dm ** (-0.5)), 'Wk': _w((lambda *s: torch.randn(*s, dtype=torch.bfloat16) * 1.6)(Dm, H, Dh) * Dm ** (-0.5)), 'Wv': _w((lambda *s: torch.randn(*s, dtype=torch.bfloat16) * 0.2)(Dm, H, Dh) * Dm ** (-0.5)), 'Wo': _w((lambda *s: torch.randn(*s, dtype=torch.bfloat16) * 0.2)(H, Dh, Dm) * (H * Dh) ** (-0.5)), 'seg_len': seg}
    _A_in = _A_setup()

    def _B_setup():
        B, S, D, F = (1, 2048, 640, 1728)
        return {'x': _w((lambda *s: torch.randn(*s, dtype=torch.bfloat16) * 0.05)(B, S, D)), 'W_gate': _w((lambda *s: torch.randn(*s, dtype=torch.bfloat16) * 0.2)(D, F) * (2.0 / D) ** 0.5), 'W_up': _w((lambda *s: torch.randn(*s, dtype=torch.bfloat16) * 0.2)(D, F) * (2.0 / D) ** 0.5), 'W_down': _w((lambda *s: torch.randn(*s, dtype=torch.bfloat16) * 0.2)(F, D) * (1.0 / F) ** 0.5)}
    _B_in = _B_setup()
    return {'A_x': _A_in['x'], 'A_mem0': _A_in['mem0'], 'A_Wq': _A_in['Wq'], 'A_Wk': _A_in['Wk'], 'A_Wv': _A_in['Wv'], 'A_Wo': _A_in['Wo'], 'A_seg_len': _A_in['seg_len'], 'B_x': _B_in['x'], 'B_W_gate': _B_in['W_gate'], 'B_W_up': _B_in['W_up'], 'B_W_down': _B_in['W_down']}

def reference(A_x, A_mem0, A_Wq, A_Wk, A_Wv, A_Wo, A_seg_len, B_x, B_W_gate, B_W_up, B_W_down):

    def _A_ref(x, mem0, Wq, Wk, Wv, Wo, seg_len):
        B, S, Dm = x.shape
        H, Dh = (Wq.shape[1], Wq.shape[2])
        M = mem0.shape[1]
        scale = Dh ** (-0.5)
        n_seg = S // seg_len
        L = M + seg_len
        idx = torch.arange(L)
        mask = (idx[:, None] < M) | (idx[:, None] >= idx[None, :])
        mem = mem0
        outs = []
        for s in range(n_seg):
            xs = x[:, s * seg_len:(s + 1) * seg_len, :].clone()
            h = torch.concatenate([mem, xs], axis=1)
            q = torch.einsum('btd,dhe->bhte', h, Wq)
            k = torch.einsum('btd,dhe->bhte', h, Wk)
            v = torch.einsum('btd,dhe->bhte', h, Wv)
            scores = torch.einsum('bhqe,bhke->bhqk', q, k) * scale
            scores = torch.where(mask[None, None], scores, -1000000000.0)
            p = _softmax(scores, axis=-1)
            o = torch.einsum('bhqk,bhke->bhqe', p, v)
            o = torch.einsum('bhte,hed->btd', o, Wo)
            mem = o[:, :M, :].clone()
            outs.append(o[:, M:, :])
        return torch.concatenate(outs, axis=1)

    def _B_ref(x, W_gate, W_up, W_down):
        gate = torch.einsum('bsd,df->bsf', x, W_gate)
        up = torch.einsum('bsd,df->bsf', x, W_up)
        gate = gate * (lambda x, dt: x.to(dt))(gate > 0, gate.dtype)
        h = gate * up
        return torch.einsum('bsf,fd->bsd', h, W_down)
    x = A_x
    xn = _layernorm(x, axis=-1)
    return x + _A_ref(xn, A_mem0, A_Wq, A_Wk, A_Wv, A_Wo, A_seg_len) + _B_ref(xn, B_W_gate, B_W_up, B_W_down)
```

```cpp
#include <hip/hip_runtime.h>
#include <hip/hip_bf16.h>

typedef unsigned u32;
typedef unsigned long long u64;
typedef u32 u32x2 __attribute__((ext_vector_type(2)));
typedef u32 u32x4 __attribute__((ext_vector_type(4)));
typedef u32 u32x8 __attribute__((ext_vector_type(8)));
typedef float f32x2 __attribute__((ext_vector_type(2)));
typedef float f32x4 __attribute__((ext_vector_type(4)));
typedef short s16x4 __attribute__((ext_vector_type(4)));
typedef __bf16 bf16x2 __attribute__((ext_vector_type(2)));
#define LAS __attribute__((address_space(3)))
#define GAS __attribute__((address_space(1)))
#define RLX_AGENT __ATOMIC_RELAXED, __HIP_MEMORY_SCOPE_AGENT
typedef __amdgpu_buffer_rsrc_t rsrc_t;

#define STAMPS 0

constexpr int NSEG = 64, DM = 640, NH = 32, FF = 1728;
constexpr u32 SENT = 0xFFFFFFFFu;
constexpr int OOB = 0x7F000000;
constexpr int AUX_SC1 = 16;
constexpr u32 SPIN_CAP = 400000;

constexpr size_t OFF_RING = 0;
constexpr size_t OFF_CAL  = 4 * 65536;
constexpr size_t OFF_FLAG = OFF_CAL + 65536;
constexpr size_t OFF_DBG  = OFF_FLAG + 16384;
constexpr size_t OFF_XF   = 1u << 20;
constexpr size_t OFF_WQ   = OFF_XF + (size_t)NSEG * 40960;
constexpr size_t OFF_WK   = OFF_WQ + 5242880;
constexpr size_t OFF_WV   = OFF_WK + 5242880;
constexpr size_t OFF_WO   = OFF_WV + 5242880;
constexpr size_t OFF_WG   = OFF_WO + 5242880;
constexpr size_t OFF_WU   = OFF_WG + 2211840;
constexpr size_t OFF_WD   = OFF_WU + 2211840;
constexpr size_t OFF_KX   = OFF_WD + 2211840;
constexpr size_t OFF_MB   = OFF_KX + (size_t)NSEG * NH * 8192;
constexpr size_t OFF_KM   = OFF_MB + (size_t)NSEG * 327680;
constexpr size_t OFF_PX   = OFF_KM + (size_t)NSEG * NH * 2048;
constexpr size_t OFF_PM   = OFF_PX + (size_t)NSEG * 65536;
constexpr size_t OFF_HF   = OFF_PM + (size_t)NSEG * 16384;
constexpr size_t OFF_U    = OFF_HF + (size_t)NSEG * 110592;
constexpr size_t WS_END   = OFF_U + (size_t)NSEG * 1310720;
static_assert(WS_END + (2u << 20) <= (256u << 20), "workspace");
constexpr int F_K = 0, F_U = 64, F_KM = 128, F_M = 192, F_PX = 256, F_H = 320  , F_SEL = 448, F_TMO = 512;

__device__ __forceinline__ int xp(int o) { return ((o >> 13) << 14) | (o & 0x1fff); }
__device__ __forceinline__ rsrc_t mk(const void* p, u32 bytes) { return __builtin_amdgcn_make_buffer_rsrc((void*)p, 0, bytes, 0x00020000); }
__device__ __forceinline__ u32x4 ld4(rsrc_t r, int off, int so = 0) { return __builtin_amdgcn_raw_buffer_load_b128(r, off, so, AUX_SC1); }
__device__ __forceinline__ u32x2 ld2(rsrc_t r, int off, int so = 0) { return __builtin_amdgcn_raw_buffer_load_b64(r, off, so, AUX_SC1); }
__device__ __forceinline__ u32x4 ld4p(rsrc_t r, int off, int so = 0) { return __builtin_amdgcn_raw_buffer_load_b128(r, off, so, 0); }
__device__ __forceinline__ void st4(rsrc_t r, int off, int so, u32x4 v) { __builtin_amdgcn_raw_buffer_store_b128(v, r, off, so, AUX_SC1); }
__device__ __forceinline__ void st2(rsrc_t r, int off, int so, u32x2 v) { __builtin_amdgcn_raw_buffer_store_b64(v, r, off, so, AUX_SC1); }
__device__ __forceinline__ void st1(rsrc_t r, int off, int so, u32 v) { __builtin_amdgcn_raw_buffer_store_b32(v, r, off, so, AUX_SC1); }
__device__ __forceinline__ u32 pk(float a, float b) { f32x2 f = {a, b}; return __builtin_bit_cast(u32, __builtin_convertvector(f, bf16x2)); }
__device__ __forceinline__ u32 pks(float a, float b) { u32 v = pk(a, b); return v < 0xFFFFFFFEu ? v : 0xFFFFFFFEu; }
__device__ __forceinline__ float bflo(u32 v) { return __builtin_bit_cast(float, v << 16); }
__device__ __forceinline__ float bfhi(u32 v) { return __builtin_bit_cast(float, v & 0xffff0000u); }
__device__ __forceinline__ float rbf(float a) { return bflo(pk(a, a) & 0xffffu); }
__device__ __forceinline__ u32x8 cat(u32x4 a, u32x4 b) { return __builtin_shufflevector(a, b, 0, 1, 2, 3, 4, 5, 6, 7); }
__device__ __forceinline__ bool ok4(u32x4 v) { return (v.x != SENT) & (v.y != SENT) & (v.z != SENT) & (v.w != SENT); }
__device__ __forceinline__ bool ok2(u32x2 v) { return (v.x != SENT) & (v.y != SENT); }
__device__ __forceinline__ u32x2 trd(LAS unsigned char* p) { return __builtin_bit_cast(u32x2, __builtin_amdgcn_ds_read_tr16_b64_v4i16((LAS s16x4*)p)); }
__device__ __forceinline__ float fb(u32 v) { return __builtin_bit_cast(float, v); }
__device__ __forceinline__ u32 bf(float v) { return __builtin_bit_cast(u32, v); }
__device__ __forceinline__ float hsum(float a, float b) { auto r = __builtin_amdgcn_permlane32_swap(bf(a), bf(b), false, false); return fb(r[0]) + fb(r[1]); }
__device__ __forceinline__ float max16(float x) { auto r = __builtin_amdgcn_permlane16_swap(bf(x), bf(x), false, false); return fmaxf(fb(r[0]), fb(r[1])); }
__device__ __forceinline__ float max32(float x) { auto r = __builtin_amdgcn_permlane32_swap(bf(x), bf(x), false, false); return fmaxf(fb(r[0]), fb(r[1])); }
__device__ __forceinline__ float add16(float x) { auto r = __builtin_amdgcn_permlane16_swap(bf(x), bf(x), false, false); return fb(r[0]) + fb(r[1]); }
__device__ __forceinline__ float add32(float x) { auto r = __builtin_amdgcn_permlane32_swap(bf(x), bf(x), false, false); return fb(r[0]) + fb(r[1]); }
__device__ __forceinline__ float ror8(float x) { return fb(__builtin_amdgcn_update_dpp(0, (int)bf(x), 0x128, 0xf, 0xf, false)); }
__device__ __forceinline__ float mov_hi8(float old, float src) { return fb(__builtin_amdgcn_update_dpp((int)bf(old), (int)bf(src), 0x128, 0xf, 0xc, false)); }

#define BAR() do { asm volatile("s_waitcnt lgkmcnt(0)" ::: "memory"); __builtin_amdgcn_s_barrier(); asm volatile("" ::: "memory"); } while (0)
#define VMCNT(n) asm volatile("s_waitcnt vmcnt(" #n ")" ::: "memory")
#define SMF_A(c, a, w, idx) asm("v_smfmac_f32_16x16x64_bf16 %0, %1, %2, %3" : "+v"(c) : "v"(a), "a"(w), "v"(idx))
#define SMF_V(c, a, w, idx) asm("v_smfmac_f32_16x16x64_bf16 %0, %1, %2, %3" : "+v"(c) : "v"(a), "v"(w), "v"(idx))
#define MF_AV(c, a, b) asm("v_mfma_f32_16x16x32_bf16 %0, %1, %2, %0" : "+v"(c) : "a"(a), "v"(b))
#define MF_VA(c, a, b) asm("v_mfma_f32_16x16x32_bf16 %0, %1, %2, %0" : "+v"(c) : "v"(a), "a"(b))
#define MF_VV(c, a, b) asm("v_mfma_f32_16x16x32_bf16 %0, %1, %2, %0" : "+v"(c) : "v"(a), "v"(b))
#define PRE2(a, b) asm volatile("s_nop 1" : "+v"(a), "+v"(b))
#define PRE3(a, b, c) asm volatile("s_nop 1" : "+v"(a), "+v"(b), "+v"(c))
#define PRE4(a, b, c, d) asm volatile("s_nop 1" : "+v"(a), "+v"(b), "+v"(c), "+v"(d))
#define POST2(a, b) asm volatile("s_nop 7" : "+v"(a), "+v"(b))
#define POST3(a, b, c) asm volatile("s_nop 7" : "+v"(a), "+v"(b), "+v"(c))
#define POST4(a, b, c, d) asm volatile("s_nop 7" : "+v"(a), "+v"(b), "+v"(c), "+v"(d))
#define Z4 (f32x4){0.f, 0.f, 0.f, 0.f}

#define STAMP(role_slot, s, k) do {} while (0)

__device__ __forceinline__ u32 wait_ge(GAS u32* word, bool active, u32 want, GAS u32* tmo, bool& dead, int sleep) {
    u32 v = want;
    for (u32 spins = 0; !dead; ++spins) {
        if (active) v = __hip_atomic_load(word, RLX_AGENT);
        if (__all(v >= want)) break;
        if (spins > SPIN_CAP) { __hip_atomic_store(tmo, 1u, RLX_AGENT); dead = true; }
        else if ((spins & 1023u) == 1023u) dead = __builtin_amdgcn_readfirstlane(__hip_atomic_load(tmo, RLX_AGENT)) != 0;
        if (sleep) __builtin_amdgcn_s_sleep(4);
    }
    __builtin_amdgcn_fence(__ATOMIC_ACQUIRE, "wavefront");
    return v;
}
__device__ __forceinline__ bool spin_fail(u32& spins, GAS u32* tmo) {
    if (++spins > SPIN_CAP) { __hip_atomic_store(tmo, 1u, RLX_AGENT); return true; }
    if ((spins & 1023u) == 0u) return __builtin_amdgcn_readfirstlane(__hip_atomic_load(tmo, RLX_AGENT)) != 0;
    return false;
}
__device__ __forceinline__ u32 wave_min(u32 v) {
    v = min(v, (u32)__builtin_amdgcn_update_dpp(-1, (int)v, 0x111, 0xf, 0xf, false));
    v = min(v, (u32)__builtin_amdgcn_update_dpp(-1, (int)v, 0x112, 0xf, 0xf, false));
    v = min(v, (u32)__builtin_amdgcn_update_dpp(-1, (int)v, 0x114, 0xf, 0xf, false));
    return (u32)__builtin_amdgcn_readlane((int)v, 7);
}

constexpr int PB_LN = 128, PB_MEM = 1, PB_FILL = 47;
constexpr int NU_QKV = 3 * 20 * 64, NU_O = 128 * 10, NU_GU = 2 * 20 * 27, NU_D = 54 * 10, NU_ALL = NU_QKV + NU_O + NU_GU + NU_D;
constexpr int PB_TILE = NU_ALL / 4;
static_assert(NU_ALL % 4 == 0, "units");
constexpr int PB_ALL = PB_LN + PB_MEM + PB_FILL + PB_TILE;

__global__ void __launch_bounds__(256) k_prep(const u32* __restrict__ x, const u32* __restrict__ mem0, const unsigned short* __restrict__ wq,
                                              const unsigned short* __restrict__ wk, const unsigned short* __restrict__ wv, const unsigned short* __restrict__ wo,
                                              const unsigned short* __restrict__ wg, const unsigned short* __restrict__ wu, const unsigned short* __restrict__ wd,
                                              unsigned char* __restrict__ base) {
    __shared__ __attribute__((aligned(16))) unsigned char lds[4 * 32 * 144];
    const int tid = threadIdx.x, lane = tid & 63, wave = __builtin_amdgcn_readfirstlane(tid >> 6);
    int b = blockIdx.x;
    if (b < PB_LN) {
        if (b == 1 && wave == 0) {
            GAS u32* cal = (GAS u32*)(base + OFF_CAL);
            if (lane < 16) __hip_atomic_store(cal + (lane & 1) * 2048 + (lane >> 1) * 64, 0u, RLX_AGENT);
            VMCNT(0);
            u32 best[2] = {~0u, ~0u};
#pragma unroll 1
            for (int i = 0; i < 16; ++i) {
                GAS u32* p = cal + (i & 1) * 2048 + (i >> 1) * 64;
                u64 t0, t1; u32 v;
                asm volatile("s_memtime %0\n s_waitcnt lgkmcnt(0)\n global_load_dword %2, %3, off sc1\n s_waitcnt vmcnt(0)\n s_memtime %1\n s_waitcnt lgkmcnt(0)"
                             : "=&s"(t0), "=&s"(t1), "=&v"(v) : "v"(p) : "memory");
                const u32 dt = (u32)(t1 - t0);
                if (i & 1) best[1] = min(best[1], dt); else best[0] = min(best[0], dt);
            }
            if (lane == 0) ((GAS u32*)(base + OFF_FLAG))[F_SEL] = best[1] < best[0] ? 8192u : 0u;
        }
        const int tok = tid >> 4, c = tid & 15;
        const u32x4* row = (const u32x4*)(x + (size_t)(b * 16 + tok) * (DM / 2));
        u32x4 v[5];
#pragma unroll
        for (int i = 0; i < 5; ++i) v[i] = row[c + 16 * i];
        float s = 0.f;
#pragma unroll
        for (int i = 0; i < 5; ++i)
#pragma unroll
            for (int j = 0; j < 4; ++j) s += bflo(v[i][j]) + bfhi(v[i][j]);
        s += __shfl_xor(s, 1); s += __shfl_xor(s, 2); s += __shfl_xor(s, 4); s += __shfl_xor(s, 8);
        const float mean = s * (1.f / DM);
        float q = 0.f;
#pragma unroll
        for (int i = 0; i < 5; ++i)
#pragma unroll
            for (int j = 0; j < 4; ++j) { const float d0 = bflo(v[i][j]) - mean, d1 = bfhi(v[i][j]) - mean; q += d0 * d0 + d1 * d1; }
        q += __shfl_xor(q, 1); q += __shfl_xor(q, 2); q += __shfl_xor(q, 4); q += __shfl_xor(q, 8);
        const float mb = rbf(mean), den = rbf(sqrtf(rbf(rbf(q * (1.f / DM)) + 1e-05f)));
        unsigned char* dst = base + OFF_XF + (size_t)(b >> 1) * 40960 + (b & 1) * 1024;
#pragma unroll
        for (int i = 0; i < 5; ++i) {
            u32x4 o;
#pragma unroll
            for (int j = 0; j < 4; ++j) o[j] = pk(rbf(bflo(v[i][j]) - mb) / den, rbf(bfhi(v[i][j]) - mb) / den);
            const int ch = c + 16 * i;
            *(u32x4*)(dst + (ch >> 2) * 2048 + ((ch & 3) * 16 + tok) * 16) = o;
        }
        return;
    }
    b -= PB_LN;
    if (b < PB_MEM) {
        u32* fl = (u32*)(base + OFF_FLAG);
        for (int i = tid; i < 1024; i += 256) if (i != F_SEL) fl[i] = 0u;
        for (int i = tid; i < 2560; i += 256) {
            const int cs = i >> 6, r = (i >> 2) & 15, q = i & 3;
            u32 v = mem0[((r & 7) * DM + cs * 16 + 4 * q + 2 * (r >> 3)) >> 1];
            v = v < 0xFFFFFFFEu ? v : 0xFFFFFFFEu;
            const int o = xp(20480 + i * 4);
            *(u32*)(base + OFF_RING + o) = v;
            *(u32*)(base + OFF_RING + o + 8192) = v;
        }
        return;
    }
    b -= PB_MEM;
    if (b < PB_FILL) {
        const u32x4 sv = {SENT, SENT, SENT, SENT};
        for (int i = b * 256 + tid; i < 16384 + 40960; i += PB_FILL * 256) {
            if (i < 16384) {
                const int o = i * 16, lo = ((o >> 14) << 13) | (o & 0x1fff);
                if (lo >= 20480 && lo < 30720) continue;
                *(u32x4*)(base + OFF_RING + o) = sv;
            } else {
                *(u32x4*)(base + OFF_MB + (size_t)(i - 16384) * 16) = sv;
            }
        }
        return;
    }
    b -= PB_FILL;
    int u = b * 4 + wave;
    const unsigned short* src; int ld, kb, nb, perm; size_t dst, sn, sk;
    if (u < NU_QKV) { const int m = u / 1280, r = u % 1280; src = m == 0 ? wq : (m == 1 ? wk : wv); ld = 4096; kb = r / 64; nb = r % 64; perm = 0;
        dst = OFF_WQ + (size_t)m * 5242880 + (size_t)kb * 1024; sn = 20480; sk = 0; }
    else if ((u -= NU_QKV) < NU_O) { src = wo; ld = DM; kb = u / 10; nb = u % 10; perm = 1; dst = OFF_WO + (size_t)(kb >> 2) * 163840 + (kb & 3) * 1024; sn = 4096; sk = 0; }
    else if ((u -= NU_O) < NU_GU) { const int m = u / 540, r = u % 540; src = m == 0 ? wg : wu; ld = FF; kb = r / 27; nb = r % 27; perm = 0;
        dst = OFF_WG + (size_t)m * 2211840 + (size_t)kb * 1024; sn = 20480; sk = 0; }
    else { u -= NU_GU; src = wd; ld = DM; kb = u / 10; nb = u % 10; perm = 1; dst = OFF_WD + (size_t)kb * 1024; sn = 54 * 1024; sk = 0; }
    (void)sk;
    LAS unsigned char* img = (LAS unsigned char*)lds + wave * (32 * 144);
    u32x4 t[4];
#pragma unroll
    for (int i = 0; i < 4; ++i) t[i] = *(const u32x4*)(src + (size_t)(kb * 32 + i * 8 + (lane >> 3)) * ld + nb * 64 + (lane & 7) * 8);
#pragma unroll
    for (int i = 0; i < 4; ++i) *(LAS u32x4*)(img + (i * 8 + (lane >> 3)) * 144 + (lane & 7) * 16) = t[i];
    asm volatile("s_waitcnt lgkmcnt(0)" ::: "memory");
    const int g = lane >> 4, qq = (lane >> 2) & 3, p = lane & 3;
    const int r0 = perm ? 4 * g + qq : 8 * g + qq, r1 = perm ? 16 + 4 * g + qq : 8 * g + 4 + qq;
#pragma unroll
    for (int i = 0; i < 4; ++i) {
        const u32x2 a = trd(img + r0 * 144 + i * 32 + p * 8), c = trd(img + r1 * 144 + i * 32 + p * 8);
        *(u32x4*)(base + dst + (size_t)(nb * 4 + i) * sn + lane * 16) = (u32x4){a.x, a.y, c.x, c.y};
    }
}

constexpr int L_MEM = 0, L_IMG = 10240, L_RED = 14336;

template <bool IS_A>
__device__ __forceinline__ void chain_role(unsigned char* base, LAS unsigned char* L, const int h, const int cs) {
    const int tid = threadIdx.x, lane = tid & 63, w = __builtin_amdgcn_readfirstlane(tid >> 6);
    const int n = lane & 15, g = lane >> 4;
    GAS u32* flags = (GAS u32*)(base + OFF_FLAG);
    GAS u32* tmo = flags + F_TMO;
    const int sel = __builtin_amdgcn_readfirstlane((int)flags[F_SEL]);
    const rsrc_t RR = mk(base + OFF_RING, 4 * 65536);
    bool dead = false;
    u32x8 wt[40];
    {
        const rsrc_t RW = mk(base + OFF_WQ, 4 * 5242880);
        if constexpr (IS_A) {
#pragma unroll
            for (int j = 0; j < 10; ++j)
#pragma unroll
                for (int t = 0; t < 4; ++t) {
                    const int so = (t >> 1) * 5242880 + ((h * 8 + 2 * w + (t & 1)) * 20 + 2 * j) * 1024;
                    wt[j * 4 + t] = cat(ld4p(RW, lane * 16, so), ld4p(RW, lane * 16 + 1024, so));
                }
        } else {
#pragma unroll
            for (int j = 0; j < 10; ++j)
#pragma unroll
                for (int t = 0; t < 2; ++t) {
                    const int so = 2 * 5242880 + ((h * 8 + 2 * w + t) * 20 + 2 * j) * 1024;
                    wt[j * 2 + t] = cat(ld4p(RW, lane * 16, so), ld4p(RW, lane * 16 + 1024, so));
                }
#pragma unroll
            for (int i = 0; i < 10; ++i)
#pragma unroll
                for (int t = 0; t < 2; ++t) {
                    const int so = 3 * 5242880 + h * 163840 + (10 * w + i) * 4096 + t * 2048;
                    wt[20 + t * 10 + i] = cat(ld4p(RW, lane * 16, so), ld4p(RW, lane * 16 + 1024, so));
                }
        }
    }
    const u32 idx = (lane & 8) ? 0xEEEEu : 0x4444u;
    const int mo0 = xp(20480 + w * 1024 + lane * 16), mo1 = xp(20480 + (w + 4) * 1024 + lane * 16), mo2 = w < 2 ? xp(20480 + (w + 8) * 1024 + lane * 16) : OOB;
    const u32x4 sent4 = {SENT, SENT, SENT, SENT};
    u32 knownU = 0, knownK = 0;
    u32x4 kx[8];
    if constexpr (IS_A) {
        if (w == 0) {
            knownK = (u32)__builtin_amdgcn_readfirstlane((int)wait_ge(flags + F_K + h, true, 1u, tmo, dead, 0));
            const rsrc_t RK = mk(base + OFF_KX + (size_t)h * 8192, 8192);
#pragma unroll
            for (int i = 0; i < 8; ++i) kx[i] = ld4(RK, lane * 16 + i * 1024);
        }
    }
#pragma unroll 1
    for (int s = 0; s < NSEG; ++s) {
        const int slot = (s & 3) * 65536 + sel;
        {
            u32x4 g0, g1, g2;
            for (u32 spins = 0;;) {
                asm volatile("" ::: "memory");
                g0 = ld4(RR, mo0, slot); g1 = ld4(RR, mo1, slot); g2 = ld4(RR, mo2, slot);
                if (__all(ok4(g0) & ok4(g1) & ok4(g2))) break;
                if (dead || spin_fail(spins, tmo)) { dead = true; break; }
            }
            *(LAS u32x4*)(L + L_MEM + w * 1024 + lane * 16) = g0;
            *(LAS u32x4*)(L + L_MEM + (w + 4) * 1024 + lane * 16) = g1;
            if (w < 2) *(LAS u32x4*)(L + L_MEM + (w + 8) * 1024 + lane * 16) = g2;
        }
        BAR();
        if (w == 0) STAMP(0, s, 0);
        u32x4 a[10];
#pragma unroll
        for (int j = 0; j < 10; ++j) a[j] = *(LAS u32x4*)(L + L_MEM + j * 1024 + lane * 16);
        if (s > 0 && tid == 0) __hip_atomic_store(flags + (IS_A ? F_KM : F_M) + h, (u32)s, RLX_AGENT);
        u32x8 uf[4];
        const bool doC = cs >= 0 && s < NSEG - 1;
        if (doC) {
            if (knownU <= (u32)s) knownU = wave_min(wait_ge(flags + F_U + 8 * w + (lane & 7), true, (u32)s + 1u, tmo, dead, 0));
            const rsrc_t RU = mk(base + OFF_U + (size_t)s * 1310720 + (size_t)cs * 32768 + w * 8192, 8192);
#pragma unroll
            for (int t = 0; t < 4; ++t) uf[t] = cat(ld4(RU, lane * 16 + t * 2048), ld4(RU, lane * 16 + t * 2048 + 1024));
        }
        const rsrc_t RM = mk(base + OFF_MB + (size_t)s * 327680, 327680);
        if constexpr (IS_A) {
            f32x4 c0 = Z4, c1 = Z4, c2 = Z4, c3 = Z4;
            PRE4(c0, c1, c2, c3);
#pragma unroll
            for (int j = 0; j < 10; ++j) {
                if (j < 8) { SMF_A(c0, a[j], wt[j * 4], idx); SMF_A(c2, a[j], wt[j * 4 + 2], idx); SMF_A(c1, a[j], wt[j * 4 + 1], idx); SMF_A(c3, a[j], wt[j * 4 + 3], idx); }
                else { SMF_V(c0, a[j], wt[j * 4], idx); SMF_V(c2, a[j], wt[j * 4 + 2], idx); SMF_V(c1, a[j], wt[j * 4 + 1], idx); SMF_V(c3, a[j], wt[j * 4 + 3], idx); }
            }
            POST4(c0, c1, c2, c3);
            *(LAS u32x2*)(L + L_IMG + (32 * w + n) * 32 + 8 * g) = (u32x2){pk(hsum(c0[0], c2[0]), hsum(c0[1], c2[1])), pk(hsum(c0[2], c2[2]), hsum(c0[3], c2[3]))};
            *(LAS u32x2*)(L + L_IMG + (32 * w + 16 + n) * 32 + 8 * g) = (u32x2){pk(hsum(c1[0], c3[0]), hsum(c1[1], c3[1])), pk(hsum(c1[2], c3[2]), hsum(c1[3], c3[3]))};
            BAR();
            if (w == 0) {
                STAMP(0, s, 1);
                u32x4 X[4];
                LAS unsigned char* ip = L + L_IMG + (4 * g + (n >> 2)) * 32 + (n & 3) * 8;
#pragma unroll
                for (int e = 0; e < 4; ++e) { const u32x2 lo = trd(ip + e * 1024), hi = trd(ip + e * 1024 + 512); X[e] = (u32x4){lo.x, lo.y, hi.x, hi.y}; }
                if (s < NSEG - 1) {
                    f32x4 t0 = Z4, t1 = Z4, t2 = Z4;
                    PRE3(t0, t1, t2);
#pragma unroll
                    for (int e = 0; e < 4; ++e) { MF_VV(t1, kx[e], X[e]); MF_VV(t2, kx[4 + e], X[e]); MF_VV(t0, X[e], X[e]); }
                    POST3(t0, t1, t2);
                    float y[6];
#pragma unroll
                    for (int r = 0; r < 4; ++r) y[r] = mov_hi8(t1[r], t2[r]);
                    y[4] = mov_hi8(t0[0], t0[2]); y[5] = mov_hi8(t0[1], t0[3]);
                    if (g < 2) { y[4] = -INFINITY; y[5] = -INFINITY; }
                    float mx = fmaxf(fmaxf(fmaxf(y[0], y[1]), fmaxf(y[2], y[3])), fmaxf(y[4], y[5]));
                    mx = fmaxf(mx, ror8(mx)); mx = max16(mx); mx = max32(mx);
                    const float cf = 0.08838834764831845f * 1.4426950408889634f, mc = mx * cf;
                    float sum = 0.f;
#pragma unroll
                    for (int r = 0; r < 6; ++r) { y[r] = __builtin_amdgcn_exp2f(y[r] * cf - mc); sum += y[r]; }
                    sum += ror8(sum); sum = add16(sum); sum = add32(sum);
                    const float rs = __builtin_amdgcn_rcpf(sum);
                    st2(RR, xp(h * 512 + g * 128 + (n & 7) * 16 + (n >> 3) * 8), slot, (u32x2){pks(y[0] * rs, y[1] * rs), pks(y[2] * rs, y[3] * rs)});
                    st1(RR, g >= 2 ? xp(16384 + h * 128 + n * 8 + (g - 2) * 4) : OOB, slot, pks(y[4] * rs, y[5] * rs));
                    STAMP(0, s, 2);
                }
                const rsrc_t RKM = mk(base + OFF_KM + ((size_t)s * NH + h) * 2048, 2048);
#pragma unroll
                for (int e = 0; e < 4; ++e) st4(RKM, n >= 8 ? e * 512 + (g * 8 + n - 8) * 16 : OOB, 0, X[e]);
                if (s < NSEG - 1) {
                    st4(RR, lane < 40 ? xp(lane < 32 ? h * 512 + lane * 16 : 16384 + h * 128 + (lane - 32) * 16) : OOB, ((s + 2) & 3) * 65536 + sel, sent4);
                    if (knownK <= (u32)s + 1u) knownK = (u32)__builtin_amdgcn_readfirstlane((int)wait_ge(flags + F_K + h, true, (u32)s + 2u, tmo, dead, 0));
                    const rsrc_t RK = mk(base + OFF_KX + ((size_t)(s + 1) * NH + h) * 8192, 8192);
#pragma unroll
                    for (int i = 0; i < 8; ++i) kx[i] = ld4(RK, lane * 16 + i * 1024);
                }
            }
        } else {
            f32x4 c0 = Z4, c1 = Z4;
            PRE2(c0, c1);
#pragma unroll
            for (int j = 0; j < 10; ++j) { SMF_A(c0, a[j], wt[j * 2], idx); SMF_A(c1, a[j], wt[j * 2 + 1], idx); }
            POST2(c0, c1);
            *(LAS u32x2*)(L + L_IMG + (32 * w + 16 * (g >> 1) + n) * 16 + 8 * (g & 1)) =
                (u32x2){pk(hsum(c0[0], c1[0]), hsum(c0[1], c1[1])), pk(hsum(c0[2], c1[2]), hsum(c0[3], c1[3]))};
            BAR();
            if (w == 0) STAMP(0, s, 1);
            u32x4 av[2];
            {
                const int q = n >> 2, p = n & 3;
                LAS unsigned char* ip = L + L_IMG + (32 * (g >> 1) + 8 * (g & 1) + (q & 1) + 16 * (q >> 1) + 2 * (p >> 1)) * 16 + 8 * (p & 1);
#pragma unroll
                for (int t = 0; t < 2; ++t) { const u32x2 lo = trd(ip + t * 1024), hi = trd(ip + t * 1024 + 64); av[t] = (u32x4){lo.x, lo.y, hi.x, hi.y}; }
            }
            f32x4 c[10];
#pragma unroll
            for (int i = 0; i < 10; ++i) c[i] = Z4;
            PRE4(c[0], c[1], c[2], c[3]); PRE4(c[4], c[5], c[6], c[7]); PRE2(c[8], c[9]);
#pragma unroll
            for (int i = 0; i < 10; i += 2) {
#pragma unroll
                for (int k = 0; k < 2; ++k) {
                    SMF_A(c[i + k], av[0], wt[20 + i + k], idx);
                }
#pragma unroll
                for (int k = 0; k < 2; ++k) {
                    if (30 + i + k < 32) SMF_A(c[i + k], av[1], wt[30 + i + k], idx); else SMF_V(c[i + k], av[1], wt[30 + i + k], idx);
                }
            }
#pragma unroll
            for (int i = 0; i < 10; i += 2) {
                POST2(c[i], c[i + 1]);
                st2(RM, (10 * w + i + (g >> 1)) * 8192 + h * 256 + n * 16 + (g & 1) * 8, 0,
                    (u32x2){pks(hsum(c[i][0], c[i + 1][0]), hsum(c[i][1], c[i + 1][1])), pks(hsum(c[i][2], c[i + 1][2]), hsum(c[i][3], c[i + 1][3]))});
            }
            if (w == 0) STAMP(0, s, 2);
            if (s + 2 < NSEG) {
                const rsrc_t RM2 = mk(base + OFF_MB + (size_t)(s + 2) * 327680, 327680);
#pragma unroll
                for (int i = 0; i < 3; ++i) st4(RM2, 4 * i + g < 10 ? (10 * w + 4 * i + g) * 8192 + h * 256 + n * 16 : OOB, 0, sent4);
            }
        }
        if (doC) {
            u32x4 m0, m1, p0, p1, p2, p3; u32x2 pa, pb;
            const int mo = cs * 8192 + w * 2048 + lane * 16, po = xp(w * 4096 + lane * 16), pmo = xp(16384 + (8 * w + 2 * g) * 128 + n * 8);
            for (u32 spins = 0;;) {
                asm volatile("" ::: "memory");
                m0 = ld4(RM, mo); m1 = ld4(RM, mo + 1024);
                if (__all(ok4(m0) & ok4(m1))) break;
                if (dead || spin_fail(spins, tmo)) { dead = true; break; }
            }
            for (u32 spins = 0;;) {
                asm volatile("" ::: "memory");
                p0 = ld4(RR, po, slot); p1 = ld4(RR, po + 1024, slot); p2 = ld4(RR, po + 2048, slot); p3 = ld4(RR, po + 3072, slot);
                pa = ld2(RR, pmo, slot); pb = ld2(RR, pmo + 128, slot);
                if (__all(ok4(p0) & ok4(p1) & ok4(p2) & ok4(p3) & ok2(pa) & ok2(pb))) break;
                if (dead || spin_fail(spins, tmo)) { dead = true; break; }
            }
            const u32x8 mf = cat(m0, m1);
            u32x4 pm = {pa.x, pa.y, pb.x, pb.y};
            f32x4 c0 = Z4, c1 = Z4;
            PRE3(c0, c1, pm);
            SMF_V(c0, p0, uf[0], idx); SMF_V(c1, p1, uf[1], idx); SMF_V(c0, p2, uf[2], idx); SMF_V(c1, p3, uf[3], idx); SMF_V(c0, pm, mf, idx);
            POST2(c0, c1);
            c0 += c1;
            LAS float* red = (LAS float*)(L + L_RED) + w * 128 + (4 * (g & 1) + (g >> 1)) * 16 + n;
            red[0] = hsum(c0[0], c0[1]);
            red[32] = hsum(c0[2], c0[3]);
            BAR();
            if (w == 0) {
                LAS unsigned char* rp = L + L_RED + (((lane >> 2) & 7) * 16 + 4 * (lane & 3) + 2 * (lane >> 5)) * 4;
                const f32x2 r0 = *(LAS f32x2*)rp, r1 = *(LAS f32x2*)(rp + 512), r2 = *(LAS f32x2*)(rp + 1024), r3 = *(LAS f32x2*)(rp + 1536);
                const f32x2 r = (r0 + r1) + (r2 + r3);
                st1(RR, xp(20480 + cs * 256 + lane * 4), ((s + 1) & 3) * 65536 + sel, pks(r.x, r.y));
                STAMP(0, s, 3);
                st4(RR, lane < 16 ? xp(20480 + cs * 256 + lane * 16) : OOB, ((s + 3) & 3) * 65536 + sel, sent4);
            }
        }
    }
    VMCNT(0);
    BAR();
    if (tid == 0) __hip_atomic_store(flags + (IS_A ? F_KM : F_M) + h, (u32)NSEG, RLX_AGENT);
}

constexpr int L_XF = 0, L_EX = 81920;

__device__ __forceinline__ void dma_xf(const unsigned char* base, int seg, int buf, int w, int lane) {
    const unsigned char* src = base + OFF_XF + (size_t)seg * 40960 + w * 10240 + lane * 16;
    const u32 dst = L_XF + buf * 40960 + w * 10240;
#pragma unroll
    for (int i = 0; i < 10; ++i) {
        u32 keep;
        asm volatile("s_mov_b32 %0, m0\n\ts_mov_b32 m0, %2\n\ts_nop 0\n\tglobal_load_lds_dwordx4 %1, off\n\ts_mov_b32 m0, %0"
                     : "=&s"(keep) : "v"(src + i * 1024), "s"(dst + i * 1024) : "memory");
    }
}
#define PROJ2(acc, WT, O, NA, xb)                                                                                        \
    do {                                                                                                                 \
        acc[0] = Z4; acc[1] = Z4; acc[2] = Z4; acc[3] = Z4;                                                              \
        PRE4(acc[0], acc[1], acc[2], acc[3]);                                                                            \
        _Pragma("unroll") for (int ks = 0; ks < 20; ++ks) {                                                              \
            const u32x4 b0 = *(LAS u32x4*)((xb) + ks * 2048), b1 = *(LAS u32x4*)((xb) + ks * 2048 + 1024);               \
            if ((O) + 2 * ks + 1 < (NA)) {                                                                               \
                MF_AV(acc[0], WT[(O) + 2 * ks], b0); MF_AV(acc[1], WT[(O) + 2 * ks + 1], b0);                            \
                MF_AV(acc[2], WT[(O) + 2 * ks], b1); MF_AV(acc[3], WT[(O) + 2 * ks + 1], b1);                            \
            } else {                                                                                                     \
                MF_VV(acc[0], WT[(O) + 2 * ks], b0); MF_VV(acc[1], WT[(O) + 2 * ks + 1], b0);                            \
                MF_VV(acc[2], WT[(O) + 2 * ks], b1); MF_VV(acc[3], WT[(O) + 2 * ks + 1], b1);                            \
            }                                                                                                            \
        }                                                                                                                \
        POST4(acc[0], acc[1], acc[2], acc[3]);                                                                           \
    } while (0)
__device__ __forceinline__ u32x4 pk8(f32x4 a, f32x4 b) { return (u32x4){pk(a[0], a[1]), pk(a[2], a[3]), pk(b[0], b[1]), pk(b[2], b[3])}; }
__device__ __forceinline__ u32x4 keyfrag(u32 a01, u32 a23, u32 b01, u32 b23) {
    auto x = __builtin_amdgcn_permlane16_swap(a01, b01, false, false);
    auto y = __builtin_amdgcn_permlane16_swap(a23, b23, false, false);
    return (u32x4){x[0], x[1], y[0], y[1]};
}

__device__ __forceinline__ void role_D(unsigned char* base, LAS unsigned char* L, const int h) {
    const int tid = threadIdx.x, lane = tid & 63, w = __builtin_amdgcn_readfirstlane(tid >> 6);
    GAS u32* flags = (GAS u32*)(base + OFF_FLAG);
    dma_xf(base, 0, 0, w, lane);
    u32x4 wt[80];
    {
        const rsrc_t RW = mk(base + OFF_WV, 2 * 5242880);
#pragma unroll
        for (int ks = 0; ks < 20; ++ks)
#pragma unroll
            for (int t = 0; t < 2; ++t) wt[2 * ks + t] = ld4p(RW, lane * 16, ((h * 8 + 2 * w + t) * 20 + ks) * 1024);
#pragma unroll
        for (int i = 0; i < 40; ++i) wt[40 + i] = ld4p(RW, lane * 16, 5242880 + h * 163840 + w * 40960 + i * 1024);
    }
    VMCNT(0);
    BAR();
#pragma unroll 1
    for (int s = 0; s < NSEG; ++s) {
        if (s + 1 < NSEG) dma_xf(base, s + 1, (s + 1) & 1, w, lane);
        f32x4 acc[4];
        LAS unsigned char* xb = L + L_XF + (s & 1) * 40960 + lane * 16;
        PROJ2(acc, wt, 0, 64, xb);
        *(LAS u32x4*)(L + L_EX + w * 1024 + lane * 16) = pk8(acc[0], acc[1]);
        *(LAS u32x4*)(L + L_EX + 4096 + w * 1024 + lane * 16) = pk8(acc[2], acc[3]);
        BAR();
        u32x4 vf[8];
#pragma unroll
        for (int i = 0; i < 8; ++i) vf[i] = *(LAS u32x4*)(L + L_EX + i * 1024 + lane * 16);
        const rsrc_t RU = mk(base + OFF_U + (size_t)s * 1310720, 1310720);
#pragma unroll
        for (int i = 0; i < 10; ++i) {
            f32x4 d0 = Z4, d1 = Z4;
            PRE2(d0, d1);
#pragma unroll
            for (int e = 0; e < 4; ++e) {
                if (40 + 4 * i + e < 64) { MF_VA(d0, vf[e], wt[40 + 4 * i + e]); MF_VA(d1, vf[4 + e], wt[40 + 4 * i + e]); }
                else { MF_VV(d0, vf[e], wt[40 + 4 * i + e]); MF_VV(d1, vf[4 + e], wt[40 + 4 * i + e]); }
            }
            POST2(d0, d1);
            st4(RU, lane * 16, (10 * w + i) * 32768 + h * 1024, keyfrag(pk(d0[0], d0[1]), pk(d0[2], d0[3]), pk(d1[0], d1[1]), pk(d1[2], d1[3])));
        }
        if (s == 0) VMCNT(0); else VMCNT(10);
        BAR();
        if (tid == 0) __hip_atomic_store(flags + F_U + h, (u32)(s == 0 ? 1 : s), RLX_AGENT);
    }
    VMCNT(0);
    BAR();
    if (tid == 0) __hip_atomic_store(flags + F_U + h, (u32)NSEG, RLX_AGENT);
}

__device__ __forceinline__ void role_E(unsigned char* base, LAS unsigned char* L, const int h, const int passes) {
    const int tid = threadIdx.x, lane = tid & 63, w = __builtin_amdgcn_readfirstlane(tid >> 6);
    const int n = lane & 15, g = lane >> 4;
    GAS u32* flags = (GAS u32*)(base + OFF_FLAG);
    GAS u32* tmo = flags + F_TMO;
    bool dead = false;
    dma_xf(base, 0, 0, w, lane);
    u32x4 wt[80];
    {
        const rsrc_t RW = mk(base + OFF_WQ, 2 * 5242880);
#pragma unroll
        for (int ks = 0; ks < 20; ++ks)
#pragma unroll
            for (int t = 0; t < 2; ++t) wt[2 * ks + t] = ld4p(RW, lane * 16, 5242880 + ((h * 8 + 2 * w + t) * 20 + ks) * 1024);
#pragma unroll
        for (int ks = 0; ks < 20; ++ks)
#pragma unroll
            for (int t = 0; t < 2; ++t) wt[40 + 2 * ks + t] = ld4p(RW, lane * 16, ((h * 8 + 2 * w + t) * 20 + ks) * 1024);
    }
    VMCNT(0);
    BAR();
    if (passes & 1) {
#pragma unroll 1
        for (int s = 0; s < NSEG; ++s) {
            dma_xf(base, (s + 1) & (NSEG - 1), (s + 1) & 1, w, lane);
            f32x4 acc[4];
            LAS unsigned char* xb = L + L_XF + (s & 1) * 40960 + lane * 16;
            PROJ2(acc, wt, 0, 64, xb);
            const rsrc_t RK = mk(base + OFF_KX + ((size_t)s * NH + h) * 8192, 8192);
            st4(RK, lane * 16, w * 1024, pk8(acc[0], acc[1]));
            st4(RK, lane * 16, 4096 + w * 1024, pk8(acc[2], acc[3]));
            if (s == 0) VMCNT(0); else VMCNT(2);
            BAR();
            if (tid == 0) __hip_atomic_store(flags + F_K + h, (u32)(s == 0 ? 1 : s), RLX_AGENT);
        }
        VMCNT(0);
        BAR();
        if (tid == 0) __hip_atomic_store(flags + F_K + h, (u32)NSEG, RLX_AGENT);
    }
    if (passes & 2) {
        u32 knownM = 0;
#pragma unroll 1
        for (int s = 0; s < NSEG; ++s) {
            if (s + 1 < NSEG) dma_xf(base, s + 1, (s + 1) & 1, w, lane);
            u32x4 kx[8];
            if (w < 2) {
                const rsrc_t RK = mk(base + OFF_KX + ((size_t)s * NH + h) * 8192, 8192);
#pragma unroll
                for (int i = 0; i < 8; ++i) kx[i] = ld4(RK, lane * 16 + i * 1024);
            }
            f32x4 acc[4];
            LAS unsigned char* xb = L + L_XF + (s & 1) * 40960 + lane * 16;
            PROJ2(acc, wt, 40, 64, xb);
            *(LAS u32x4*)(L + L_EX + w * 1024 + lane * 16) = pk8(acc[0], acc[1]);
            *(LAS u32x4*)(L + L_EX + 4096 + w * 1024 + lane * 16) = pk8(acc[2], acc[3]);
            BAR();
            if (w < 2) {
                u32x4 qf[4], km[4];
#pragma unroll
                for (int e = 0; e < 4; ++e) qf[e] = *(LAS u32x4*)(L + L_EX + w * 4096 + e * 1024 + lane * 16);
                if (knownM <= (u32)s) knownM = (u32)__builtin_amdgcn_readfirstlane((int)wait_ge(flags + F_KM + h, true, (u32)s + 1u, tmo, dead, 1));
                const rsrc_t RKM = mk(base + OFF_KM + ((size_t)s * NH + h) * 2048, 2048);
#pragma unroll
                for (int e = 0; e < 4; ++e) km[e] = ld4(RKM, n >= 8 ? e * 512 + (g * 8 + n - 8) * 16 : OOB);
                f32x4 t0 = Z4, t1 = Z4, t2 = Z4;
                PRE3(t0, t1, t2);
#pragma unroll
                for (int e = 0; e < 4; ++e) { MF_VV(t1, kx[e], qf[e]); MF_VV(t2, kx[4 + e], qf[e]); MF_VV(t0, km[e], qf[e]); }
                POST3(t0, t1, t2);
                const int tok = 16 * w + n;
                float y[12];
#pragma unroll
                for (int r = 0; r < 4; ++r) {
                    y[r] = 4 * g + r <= tok ? t1[r] : -INFINITY;
                    y[4 + r] = 16 + 4 * g + r <= tok ? t2[r] : -INFINITY;
                    y[8 + r] = g >= 2 ? t0[r] : -INFINITY;
                }
                float mx = y[0];
#pragma unroll
                for (int r = 1; r < 12; ++r) mx = fmaxf(mx, y[r]);
                mx = max16(mx); mx = max32(mx);
                const float cf = 0.08838834764831845f * 1.4426950408889634f, mc = mx * cf;
                float sum = 0.f;
#pragma unroll
                for (int r = 0; r < 12; ++r) { y[r] = __builtin_amdgcn_exp2f(y[r] * cf - mc); sum += y[r]; }
                sum = add16(sum); sum = add32(sum);
                const float rs = __builtin_amdgcn_rcpf(sum);
                const rsrc_t RP = mk(base + OFF_PX + (size_t)s * 65536 + w * 32768 + h * 1024, 1024);
                st4(RP, lane * 16, 0, keyfrag(pk(y[0] * rs, y[1] * rs), pk(y[2] * rs, y[3] * rs), pk(y[4] * rs, y[5] * rs), pk(y[6] * rs, y[7] * rs)));
                const rsrc_t RQ = mk(base + OFF_PM + (size_t)s * 16384 + w * 8192 + h * 256, 256);
                st2(RQ, g >= 2 ? n * 16 + (g - 2) * 8 : OOB, 0, (u32x2){pk(y[8] * rs, y[9] * rs), pk(y[10] * rs, y[11] * rs)});
            }
            VMCNT(0);
            BAR();
            if (tid == 0) __hip_atomic_store(flags + F_PX + h, (u32)s + 1u, RLX_AGENT);
        }
    }
}

__device__ __forceinline__ void role_G(unsigned char* base, LAS unsigned char* L, const int i) {
    const int tid = threadIdx.x, lane = tid & 63, w = __builtin_amdgcn_readfirstlane(tid >> 6);
    GAS u32* flags = (GAS u32*)(base + OFF_FLAG);
    const int set = i % 14, tg = i / 14;
    const int fs = 4 * set + w, fsc = fs < 54 ? fs : 53;
    dma_xf(base, tg, 0, w, lane);
    u32x4 wt[80];
    {
        const rsrc_t RW = mk(base + OFF_WG, 2 * 2211840);
#pragma unroll
        for (int m = 0; m < 2; ++m)
#pragma unroll
            for (int ks = 0; ks < 20; ++ks)
#pragma unroll
                for (int t = 0; t < 2; ++t) wt[40 * m + 2 * ks + t] = ld4p(RW, lane * 16, m * 2211840 + ((2 * fsc + t) * 20 + ks) * 1024);
    }
    VMCNT(0);
    BAR();
    int it = 0;
#pragma unroll 1
    for (int s = tg; s < NSEG; s += 9, ++it) {
        if (s + 9 < NSEG) dma_xf(base, s + 9, (it + 1) & 1, w, lane);
        f32x4 ga[4], ua[4];
        LAS unsigned char* xb = L + L_XF + (it & 1) * 40960 + lane * 16;
        PROJ2(ga, wt, 0, 64, xb);
        PROJ2(ua, wt, 40, 64, xb);
#pragma unroll
        for (int q = 0; q < 4; ++q)
#pragma unroll
            for (int r = 0; r < 4; ++r) { const float gt = rbf(ga[q][r]); ga[q][r] = (gt * (gt > 0.f ? 1.f : 0.f)) * rbf(ua[q][r]); }
        const rsrc_t RH = mk(base + OFF_HF + (size_t)s * 110592, 110592);
        st4(RH, fs < 54 ? lane * 16 : OOB, fsc * 1024, pk8(ga[0], ga[1]));
        st4(RH, fs < 54 ? lane * 16 : OOB, 55296 + fsc * 1024, pk8(ga[2], ga[3]));
        VMCNT(2);
        BAR();
        if (tid == 0 && it > 0) __hip_atomic_fetch_add(flags + F_H + s - 9, 1u, RLX_AGENT);
    }
    VMCNT(0);
    BAR();
    if (tid == 0) __hip_atomic_fetch_add(flags + F_H + tg + 9 * (it - 1), 1u, RLX_AGENT);
}

__device__ __forceinline__ void role_O(unsigned char* base, LAS unsigned char* L, const int i, const u32* __restrict__ x, u32* __restrict__ out) {
    const int tid = threadIdx.x, lane = tid & 63, w = __builtin_amdgcn_readfirstlane(tid >> 6);
    const int n = lane & 15, g = lane >> 4;
    GAS u32* flags = (GAS u32*)(base + OFF_FLAG);
    GAS u32* tmo = flags + F_TMO;
    bool dead = false;
#pragma unroll 1
    for (int task = i; task < NSEG * 10; task += 128) {
        const int s = task / 10, cg = task % 10;
        f32x4 acc[8];
#pragma unroll
        for (int q = 0; q < 8; ++q) acc[q] = Z4;
        PRE4(acc[0], acc[1], acc[2], acc[3]); PRE4(acc[4], acc[5], acc[6], acc[7]);
        wait_ge(flags + F_H + s, true, 14u, tmo, dead, 1);
        {
            const rsrc_t RH = mk(base + OFF_HF + (size_t)s * 110592, 110592);
            const rsrc_t RD = mk(base + OFF_WD + (size_t)cg * 4 * 55296, 4 * 55296);
#pragma unroll
            for (int k0 = 0; k0 < 14; k0 += 7) {
                u32x4 hb[14], wd[28];
#pragma unroll
                for (int k = 0; k < 7; ++k) {
                    const int fs = 14 * w + k0 + k, vo = fs < 54 ? lane * 16 : OOB;
                    hb[2 * k] = ld4(RH, vo, fs * 1024); hb[2 * k + 1] = ld4(RH, vo, 55296 + fs * 1024);
#pragma unroll
                    for (int c = 0; c < 4; ++c) wd[4 * k + c] = ld4p(RD, vo, c * 55296 + fs * 1024);
                }
#pragma unroll
                for (int k = 0; k < 7; ++k)
#pragma unroll
                    for (int c = 0; c < 4; ++c) { MF_VV(acc[2 * c], wd[4 * k + c], hb[2 * k]); MF_VV(acc[2 * c + 1], wd[4 * k + c], hb[2 * k + 1]); }
            }
        }
        wait_ge(flags + F_U + 8 * w + (lane & 7), true, (u32)s + 1u, tmo, dead, 1);
        u32x4 uf[32], mf[8];
        {
            const rsrc_t RU = mk(base + OFF_U + (size_t)s * 1310720 + (size_t)cg * 4 * 32768, 4 * 32768);
#pragma unroll
            for (int k = 0; k < 8; ++k)
#pragma unroll
                for (int c = 0; c < 4; ++c) uf[4 * k + c] = ld4(RU, lane * 16, c * 32768 + (8 * w + k) * 1024);
        }
        wait_ge(flags + F_M + 8 * w + (lane & 7), true, (u32)s + 1u, tmo, dead, 1);
        {
            const rsrc_t RM = mk(base + OFF_MB + (size_t)s * 327680 + (size_t)cg * 4 * 8192, 4 * 8192);
#pragma unroll
            for (int k = 0; k < 2; ++k)
#pragma unroll
                for (int c = 0; c < 4; ++c) mf[4 * k + c] = ld4(RM, lane * 16, c * 8192 + (2 * w + k) * 1024);
        }
        const int orow = (s * 32 + n) * (DM / 2) + (cg * 4 + w) * 8 + g * 2;
        const u32x2 x0 = *(const u32x2*)(x + orow), x1 = *(const u32x2*)(x + orow + 16 * (DM / 2));
        wait_ge(flags + F_PX + 8 * w + (lane & 7), true, (u32)s + 1u, tmo, dead, 1);
        {
            const rsrc_t RP = mk(base + OFF_PX + (size_t)s * 65536, 65536);
            const rsrc_t RQ = mk(base + OFF_PM + (size_t)s * 16384, 16384);
            u32x4 pf[16], pm[4];
#pragma unroll
            for (int k = 0; k < 8; ++k) { pf[2 * k] = ld4(RP, lane * 16, (8 * w + k) * 1024); pf[2 * k + 1] = ld4(RP, lane * 16, 32768 + (8 * w + k) * 1024); }
#pragma unroll
            for (int k = 0; k < 2; ++k) { pm[2 * k] = ld4(RQ, lane * 16, (2 * w + k) * 1024); pm[2 * k + 1] = ld4(RQ, lane * 16, 8192 + (2 * w + k) * 1024); }
#pragma unroll
            for (int k = 0; k < 8; ++k)
#pragma unroll
                for (int c = 0; c < 4; ++c) { MF_AV(acc[2 * c], uf[4 * k + c], pf[2 * k]); MF_AV(acc[2 * c + 1], uf[4 * k + c], pf[2 * k + 1]); }
#pragma unroll
            for (int k = 0; k < 2; ++k)
#pragma unroll
                for (int c = 0; c < 4; ++c) { MF_VV(acc[2 * c], mf[4 * k + c], pm[2 * k]); MF_VV(acc[2 * c + 1], mf[4 * k + c], pm[2 * k + 1]); }
        }
        POST4(acc[0], acc[1], acc[2], acc[3]); POST4(acc[4], acc[5], acc[6], acc[7]);
#pragma unroll
        for (int q = 0; q < 8; ++q) *(LAS f32x4*)(L + (w * 8 + q) * 1024 + lane * 16) = acc[q];
        BAR();
#pragma unroll
        for (int t = 0; t < 2; ++t) {
            f32x4 r = *(LAS f32x4*)(L + (2 * w + t) * 1024 + lane * 16);
#pragma unroll
            for (int k = 1; k < 4; ++k) r += *(LAS f32x4*)(L + (k * 8 + 2 * w + t) * 1024 + lane * 16);
            const u32x2 xv = t ? x1 : x0;
            *(u32x2*)(out + orow + t * 16 * (DM / 2)) = (u32x2){pk(r[0] + bflo(xv.x), r[1] + bfhi(xv.x)), pk(r[2] + bflo(xv.y), r[3] + bfhi(xv.y))};
        }
        BAR();
    }
}

constexpr int LDS_MAIN = 90112;
enum { M_CHAIN = 1, M_D = 2, M_E1 = 4, M_G = 8, M_E2 = 16, M_O = 32, M_ALL = 63 };

__global__ void __launch_bounds__(256, 1) k_main(const u32* __restrict__ x, u32* __restrict__ out, unsigned char* __restrict__ base, int mask) {
    extern __shared__ __attribute__((aligned(16))) unsigned char lds[];
    LAS unsigned char* L = (LAS unsigned char*)lds;
    const int xg = blockIdx.x & 7, j = blockIdx.x >> 3;
    if (xg == 1) { if (mask & M_CHAIN) chain_role<true>(base, L, j, j < 8 ? 32 + j : -1); }
    else if (xg == 3) { if (mask & M_CHAIN) chain_role<false>(base, L, j, j); }
    else if (xg == 0) { if (mask & M_D) role_D(base, L, j); }
    else if (xg == 2) { if (mask & (M_E1 | M_E2)) role_E(base, L, j, ((mask & M_E1) ? 1 : 0) | ((mask & M_E2) ? 2 : 0)); }
    else {
        const int i = j * 4 + (xg - 4);
        if ((mask & M_G) && i < 126) role_G(base, L, i);
        if (mask & M_O) role_O(base, L, i, x, out);
    }
}

#define SPLIT_LAUNCH 0

extern "C" void kernel_launch(void* const* d_in, const int* in_sizes, int n_in, void* d_out, int out_size, void* d_ws, size_t ws_size, hipStream_t stream) {
    (void)in_sizes; (void)n_in; (void)out_size; (void)ws_size;
    unsigned char* base = (unsigned char*)(((size_t)d_ws + (2u << 20) - 1) & ~(size_t)((2u << 20) - 1));
    (void)hipFuncSetAttribute((const void*)k_main, hipFuncAttributeMaxDynamicSharedMemorySize, LDS_MAIN);
    k_prep<<<PB_ALL, 256, 0, stream>>>((const u32*)d_in[0], (const u32*)d_in[1], (const unsigned short*)d_in[2], (const unsigned short*)d_in[3],
                                       (const unsigned short*)d_in[4], (const unsigned short*)d_in[5], (const unsigned short*)d_in[8],
                                       (const unsigned short*)d_in[9], (const unsigned short*)d_in[10], base);
    k_main<<<256, 256, LDS_MAIN, stream>>>((const u32*)d_in[0], (u32*)d_out, base, M_ALL);
}
```

```cpp
#include <hip/hip_runtime.h>
#include <hip/hip_bf16.h>

typedef unsigned u32;
typedef unsigned long long u64;
typedef u32 u32x2 __attribute__((ext_vector_type(2)));
typedef u32 u32x4 __attribute__((ext_vector_type(4)));
typedef u32 u32x8 __attribute__((ext_vector_type(8)));
typedef float f32x2 __attribute__((ext_vector_type(2)));
typedef float f32x4 __attribute__((ext_vector_type(4)));
typedef short s16x4 __attribute__((ext_vector_type(4)));
typedef __bf16 bf16x2 __attribute__((ext_vector_type(2)));
#define LAS __attribute__((address_space(3)))
#define GAS __attribute__((address_space(1)))
#define RLX_AGENT __ATOMIC_RELAXED, __HIP_MEMORY_SCOPE_AGENT
typedef __amdgpu_buffer_rsrc_t rsrc_t;

#define STAMPS 0

constexpr int NSEG = 64, DM = 640, NH = 32, FF = 1728;
constexpr u32 SENT = 0xFFFFFFFFu;
constexpr int OOB = 0x7F000000;
constexpr int AUX_SC1 = 16;
constexpr u32 SPIN_CAP = 400000;

constexpr size_t MB2 = 2u << 20;
constexpr size_t OFF_RING = 0;
constexpr size_t OFF_FLAG = 4 * 65536;
constexpr size_t OFF_DBG  = OFF_FLAG + 131072;
constexpr size_t OFF_CAL  = 3 * 655360;
constexpr int    NMBLK    = 22;
constexpr size_t OFF_MB   = MB2;
constexpr size_t OFF_XF   = OFF_MB + NMBLK * MB2;
constexpr size_t OFF_WQ   = OFF_XF + (size_t)NSEG * 40960;
constexpr size_t OFF_WK   = OFF_WQ + 5242880;
constexpr size_t OFF_WV   = OFF_WK + 5242880;
constexpr size_t OFF_WO   = OFF_WV + 5242880;
constexpr size_t OFF_WG   = OFF_WO + 5242880;
constexpr size_t OFF_WU   = OFF_WG + 2211840;
constexpr size_t OFF_WD   = OFF_WU + 2211840;
constexpr size_t OFF_KX   = OFF_WD + 2211840;
constexpr size_t OFF_KM   = OFF_KX + (size_t)NSEG * NH * 16384;
constexpr size_t OFF_PX   = OFF_KM + (size_t)NSEG * NH * 2048;
constexpr size_t OFF_PM   = OFF_PX + (size_t)NSEG * 65536;
constexpr size_t OFF_HF   = OFF_PM + (size_t)NSEG * 16384;
constexpr size_t OFF_U    = OFF_HF + (size_t)NSEG * 110592;
constexpr size_t WS_END   = OFF_U + (size_t)NSEG * 1310720;
static_assert(WS_END + MB2 <= (256u << 20), "workspace");
static_assert(OFF_DBG + 524288 + 4096 <= OFF_CAL && OFF_CAL + 16384 <= MB2, "block 0");
constexpr int F_K = 0, F_U = 64  , F_H = 320  , F_SEL = 448  , F_TMO = 512;
constexpr int F_KM = 1024;
constexpr int F_M = 2048;
constexpr int F_PX = F_M + 8192;
constexpr int F_WORDS = F_PX + 8192;
constexpr int O_BELL = 30720;
#define MAPPING 0
#define BELL_NOPS 22
#define BELL_AT 2
#define BELL2_NOPS 24
#define BELL_M 4

__device__ __forceinline__ int xp(int o) { return ((o >> 13) << 14) | (o & 0x1fff); }
__device__ __forceinline__ rsrc_t mk(const void* p, u32 bytes) { return __builtin_amdgcn_make_buffer_rsrc((void*)p, 0, bytes, 0x00020000); }
__device__ __forceinline__ u32x4 ld4(rsrc_t r, int off, int so = 0) { return __builtin_amdgcn_raw_buffer_load_b128(r, off, so, AUX_SC1); }
__device__ __forceinline__ u32x2 ld2(rsrc_t r, int off, int so = 0) { return __builtin_amdgcn_raw_buffer_load_b64(r, off, so, AUX_SC1); }
__device__ __forceinline__ u32x4 ld4p(rsrc_t r, int off, int so = 0) { return __builtin_amdgcn_raw_buffer_load_b128(r, off, so, 0); }
__device__ __forceinline__ void st4(rsrc_t r, int off, int so, u32x4 v) { __builtin_amdgcn_raw_buffer_store_b128(v, r, off, so, AUX_SC1); }
__device__ __forceinline__ void st2(rsrc_t r, int off, int so, u32x2 v) { __builtin_amdgcn_raw_buffer_store_b64(v, r, off, so, AUX_SC1); }
__device__ __forceinline__ void st1(rsrc_t r, int off, int so, u32 v) { __builtin_amdgcn_raw_buffer_store_b32(v, r, off, so, AUX_SC1); }
__device__ __forceinline__ u32 pk(float a, float b) { f32x2 f = {a, b}; return __builtin_bit_cast(u32, __builtin_convertvector(f, bf16x2)); }
__device__ __forceinline__ u32 pks(float a, float b) { u32 v = pk(a, b); return v < 0xFFFFFFFEu ? v : 0xFFFFFFFEu; }
__device__ __forceinline__ float bflo(u32 v) { return __builtin_bit_cast(float, v << 16); }
__device__ __forceinline__ float bfhi(u32 v) { return __builtin_bit_cast(float, v & 0xffff0000u); }
__device__ __forceinline__ float rbf(float a) { return bflo(pk(a, a) & 0xffffu); }
__device__ __forceinline__ u32x8 cat(u32x4 a, u32x4 b) { return __builtin_shufflevector(a, b, 0, 1, 2, 3, 4, 5, 6, 7); }
__device__ __forceinline__ bool ok4(u32x4 v) { return (v.x != SENT) & (v.y != SENT) & (v.z != SENT) & (v.w != SENT); }
__device__ __forceinline__ bool ok2(u32x2 v) { return (v.x != SENT) & (v.y != SENT); }
__device__ __forceinline__ u32x2 trd(LAS unsigned char* p) { return __builtin_bit_cast(u32x2, __builtin_amdgcn_ds_read_tr16_b64_v4i16((LAS s16x4*)p)); }
__device__ __forceinline__ float fb(u32 v) { return __builtin_bit_cast(float, v); }
__device__ __forceinline__ u32 bf(float v) { return __builtin_bit_cast(u32, v); }
__device__ __forceinline__ int rmap(int m, int hi) { return 4 * (m >> 1) + 2 * hi + (m & 1); }
__device__ __forceinline__ f32x2 fold(f32x4 c) { return (f32x2){c[0], c[1]} + (f32x2){c[2], c[3]}; }
__device__ __forceinline__ u32 pk2(f32x2 f) { return __builtin_bit_cast(u32, __builtin_convertvector(f, bf16x2)); }
__device__ __forceinline__ u32 nos(u32 v) { return v < 0xFFFFFFFEu ? v : 0xFFFFFFFEu; }
__device__ __forceinline__ float hsum(float a, float b) { auto r = __builtin_amdgcn_permlane32_swap(bf(a), bf(b), false, false); return fb(r[0]) + fb(r[1]); }
__device__ __forceinline__ float fmax3(float a, float b, float c) { float r; asm("v_max3_f32 %0, %1, %2, %3" : "=v"(r) : "v"(a), "v"(b), "v"(c)); return r; }
__device__ __forceinline__ float max16(float x) { asm volatile("s_nop 1" : "+v"(x)); auto r = __builtin_amdgcn_permlane16_swap(bf(x), bf(x), false, false); return fmax3(fb(r[0]), fb(r[1]), fb(r[1])); }
__device__ __forceinline__ float max32(float x) { asm volatile("s_nop 1" : "+v"(x)); auto r = __builtin_amdgcn_permlane32_swap(bf(x), bf(x), false, false); return fmax3(fb(r[0]), fb(r[1]), fb(r[1])); }
__device__ __forceinline__ float add16(float x) { auto r = __builtin_amdgcn_permlane16_swap(bf(x), bf(x), false, false); return fb(r[0]) + fb(r[1]); }
__device__ __forceinline__ float add32(float x) { auto r = __builtin_amdgcn_permlane32_swap(bf(x), bf(x), false, false); return fb(r[0]) + fb(r[1]); }
__device__ __forceinline__ float ror8(float x) { return fb(__builtin_amdgcn_update_dpp(0, (int)bf(x), 0x128, 0xf, 0xf, false)); }
__device__ __forceinline__ float mov_hi8(float old, float src) { return fb(__builtin_amdgcn_update_dpp((int)bf(old), (int)bf(src), 0x128, 0xf, 0xc, false)); }

#define BAR() do { asm volatile("s_waitcnt lgkmcnt(0)" ::: "memory"); __builtin_amdgcn_s_barrier(); asm volatile("" ::: "memory"); } while (0)
#define VMCNT(n) asm volatile("s_waitcnt vmcnt(" #n ")" ::: "memory")
#define SMF_A(c, a, w, idx) asm("v_smfmac_f32_16x16x64_bf16 %0, %1, %2, %3" : "+v"(c) : "v"(a), "a"(w), "v"(idx))
#define SMF_V(c, a, w, idx) asm("v_smfmac_f32_16x16x64_bf16 %0, %1, %2, %3" : "+v"(c) : "v"(a), "v"(w), "v"(idx))
#define MF_AV(c, a, b) asm("v_mfma_f32_16x16x32_bf16 %0, %1, %2, %0" : "+v"(c) : "a"(a), "v"(b))
#define MF_VA(c, a, b) asm("v_mfma_f32_16x16x32_bf16 %0, %1, %2, %0" : "+v"(c) : "v"(a), "a"(b))
#define MF_VV(c, a, b) asm("v_mfma_f32_16x16x32_bf16 %0, %1, %2, %0" : "+v"(c) : "v"(a), "v"(b))
#define MF0_AV(c, a, b) asm("v_mfma_f32_16x16x32_bf16 %0, %1, %2, 0" : "=&v"(c) : "a"(a), "v"(b))
#define MF0_VA(c, a, b) asm("v_mfma_f32_16x16x32_bf16 %0, %1, %2, 0" : "=&v"(c) : "v"(a), "a"(b))
#define MF0_VV(c, a, b) asm("v_mfma_f32_16x16x32_bf16 %0, %1, %2, 0" : "=&v"(c) : "v"(a), "v"(b))
#define SB() __builtin_amdgcn_sched_barrier(0)
#define PRE2(a, b) asm volatile("s_nop 1" : "+v"(a), "+v"(b))
#define PRE3(a, b, c) asm volatile("s_nop 1" : "+v"(a), "+v"(b), "+v"(c))
#define PRE4(a, b, c, d) asm volatile("s_nop 1" : "+v"(a), "+v"(b), "+v"(c), "+v"(d))
#define POST2(a, b) asm volatile("s_nop 7" : "+v"(a), "+v"(b))
#define POST3(a, b, c) asm volatile("s_nop 7" : "+v"(a), "+v"(b), "+v"(c))
#define POST4(a, b, c, d) asm volatile("s_nop 7" : "+v"(a), "+v"(b), "+v"(c), "+v"(d))
#define Z4 (f32x4){0.f, 0.f, 0.f, 0.f}

#define STAMP_DECL do {} while (0)
#define STAMP(k) do {} while (0)
#define STAMPW(k) do {} while (0)
#define MARK(k) do {} while (0)
#define STAMP_SET(k, v) do {} while (0)
#define STAMP_FLUSH(s) do {} while (0)

__device__ __forceinline__ u32 wait_ge(GAS u32* word, u32 want, u32& cap, bool sleep) {
    u32 v, spins = cap;
    for (;;) {
        v = __hip_atomic_load(word, RLX_AGENT);
        if (__builtin_amdgcn_ballot_w64(v < want) == 0 || --spins == 0) break;
        if (sleep) __builtin_amdgcn_s_sleep(4);
    }
    cap = spins ? cap : 1u;
    __builtin_amdgcn_fence(__ATOMIC_ACQUIRE, "wavefront");
    return v;
}
__device__ __forceinline__ u32 mx3(u32 a, u32 b, u32 c) { return max(max(a, b), c); }
__device__ __forceinline__ u32 mx4(u32 t, u32x4 v) { return mx3(mx3(t, v.x, v.y), v.z, v.w); }
__device__ __forceinline__ u64 sel_mask(GAS u32* flags, int lane) { return __builtin_amdgcn_ballot_w64(lane < 32 && flags[F_SEL + (lane & 31)] != 0u); }
__device__ __forceinline__ unsigned char* m_base(unsigned char* base, int s, u64 selm) {
    const int b = s / 3;
    return base + OFF_MB + (size_t)b * MB2 + (s - 3 * b) * 655360 + (((selm >> (b + 1)) & 1) << 13);
}
__device__ __forceinline__ u32 wave_min(u32 v) {
    v = min(v, (u32)__builtin_amdgcn_update_dpp(-1, (int)v, 0x111, 0xf, 0xf, false));
    v = min(v, (u32)__builtin_amdgcn_update_dpp(-1, (int)v, 0x112, 0xf, 0xf, false));
    v = min(v, (u32)__builtin_amdgcn_update_dpp(-1, (int)v, 0x114, 0xf, 0xf, false));
    return (u32)__builtin_amdgcn_readlane((int)v, 7);
}

constexpr int PB_LN = 128, PB_MEM = 1, PB_FILL = 47;
constexpr int NU_QKV = 3 * 20 * 64, NU_O = 128 * 10, NU_GU = 2 * 20 * 27, NU_D = 54 * 10, NU_ALL = NU_QKV + NU_O + NU_GU + NU_D;
constexpr int PB_TILE = NU_ALL / 4;
static_assert(NU_ALL % 4 == 0, "units");
constexpr int PB_ALL = PB_LN + PB_MEM + PB_FILL + PB_TILE;

__global__ void __launch_bounds__(256) k_prep(const u32* __restrict__ x, const u32* __restrict__ mem0, const unsigned short* __restrict__ wq,
                                              const unsigned short* __restrict__ wk, const unsigned short* __restrict__ wv, const unsigned short* __restrict__ wo,
                                              const unsigned short* __restrict__ wg, const unsigned short* __restrict__ wu, const unsigned short* __restrict__ wd,
                                              unsigned char* __restrict__ base) {
    __shared__ __attribute__((aligned(16))) unsigned char lds[4 * 32 * 144];
    const int tid = threadIdx.x, lane = tid & 63, wave = __builtin_amdgcn_readfirstlane(tid >> 6);
    int b = blockIdx.x;
    if (b < PB_LN) {
        if ((b & 7) == 1 && (b >> 3) * 4 + wave <= NMBLK) {
            const int blk = (b >> 3) * 4 + wave;
            GAS u32* cal = (GAS u32*)(base + (size_t)blk * MB2 + OFF_CAL);
            if (lane < 12) __hip_atomic_store(cal + (lane & 1) * 2048 + (lane >> 1) * 64, 0u, RLX_AGENT);
            VMCNT(0);
            u32 best[2] = {~0u, ~0u};
#pragma unroll 1
            for (int i = 0; i < 12; ++i) {
                GAS u32* p = cal + (i & 1) * 2048 + (i >> 1) * 64;
                u64 t0, t1; u32 v;
                asm volatile("s_memtime %0\n s_waitcnt lgkmcnt(0)\n global_load_dword %2, %3, off sc1\n s_waitcnt vmcnt(0)\n s_memtime %1\n s_waitcnt lgkmcnt(0)"
                             : "=&s"(t0), "=&s"(t1), "=&v"(v) : "v"(p) : "memory");
                const u32 dt = (u32)(t1 - t0);
                if (i & 1) best[1] = min(best[1], dt); else best[0] = min(best[0], dt);
            }
            if (lane == 0) ((GAS u32*)(base + OFF_FLAG))[F_SEL + blk] = best[1] < best[0] ? 1u : 0u;
        }
        const int tok = tid >> 4, c = tid & 15;
        const u32x4* row = (const u32x4*)(x + (size_t)(b * 16 + tok) * (DM / 2));
        u32x4 v[5];
#pragma unroll
        for (int i = 0; i < 5; ++i) v[i] = row[c + 16 * i];
        float s = 0.f;
#pragma unroll
        for (int i = 0; i < 5; ++i)
#pragma unroll
            for (int j = 0; j < 4; ++j) s += bflo(v[i][j]) + bfhi(v[i][j]);
        s += __shfl_xor(s, 1); s += __shfl_xor(s, 2); s += __shfl_xor(s, 4); s += __shfl_xor(s, 8);
        const float mean = s * (1.f / DM);
        float q = 0.f;
#pragma unroll
        for (int i = 0; i < 5; ++i)
#pragma unroll
            for (int j = 0; j < 4; ++j) { const float d0 = bflo(v[i][j]) - mean, d1 = bfhi(v[i][j]) - mean; q += d0 * d0 + d1 * d1; }
        q += __shfl_xor(q, 1); q += __shfl_xor(q, 2); q += __shfl_xor(q, 4); q += __shfl_xor(q, 8);
        const float mb = rbf(mean), den = rbf(sqrtf(rbf(rbf(q * (1.f / DM)) + 1e-05f)));
        unsigned char* dst = base + OFF_XF + (size_t)(b >> 1) * 40960 + (b & 1) * 1024;
#pragma unroll
        for (int i = 0; i < 5; ++i) {
            u32x4 o;
#pragma unroll
            for (int j = 0; j < 4; ++j) o[j] = pk(rbf(bflo(v[i][j]) - mb) / den, rbf(bfhi(v[i][j]) - mb) / den);
            const int ch = c + 16 * i;
            *(u32x4*)(dst + (ch >> 2) * 2048 + ((ch & 3) * 16 + tok) * 16) = o;
        }
        return;
    }
    b -= PB_LN;
    if (b < PB_MEM) {
        u32* fl = (u32*)(base + OFF_FLAG);
        for (int i = tid; i < F_WORDS; i += 256) if (i < F_SEL || i > F_SEL + NMBLK) fl[i] = 0u;
        for (int i = tid; i < 2560; i += 256) {
            const int cs = i >> 6, r = (i >> 2) & 15, q = i & 3;
            u32 v = mem0[((2 * (r >> 2) + (r & 1)) * DM + cs * 16 + 4 * q + (r & 2)) >> 1];
            v = v < 0xFFFFFFFEu ? v : 0xFFFFFFFEu;
            const int o = xp(20480 + i * 4);
            *(u32*)(base + OFF_RING + o) = v;
            *(u32*)(base + OFF_RING + o + 8192) = v;
        }
        return;
    }
    b -= PB_MEM;
    if (b < PB_FILL) {
        const u32x4 sv = {SENT, SENT, SENT, SENT};
        for (int i = b * 256 + tid; i < 16384 + 81920; i += PB_FILL * 256) {
            if (i < 16384) {
                const int o = i * 16, lo = ((o >> 14) << 13) | (o & 0x1fff);
                if (lo >= 20480 && lo < 30720) continue;
                *(u32x4*)(base + OFF_RING + o) = sv;
            } else {
                *(u32x4*)(base + OFF_MB + (size_t)(i - 16384) * 16) = sv;
            }
        }
        return;
    }
    b -= PB_FILL;
    int u = b * 4 + wave;
    const unsigned short* src; int ld, kb, nb, perm; size_t dst, sn, sk;
    if (u < NU_QKV) { const int m = u / 1280, r = u % 1280; src = m == 0 ? wq : (m == 1 ? wk : wv); ld = 4096; kb = r / 64; nb = r % 64; perm = 0;
        dst = OFF_WQ + (size_t)m * 5242880 + (size_t)kb * 1024; sn = 20480; sk = 0; }
    else if ((u -= NU_QKV) < NU_O) { src = wo; ld = DM; kb = u / 10; nb = u % 10; perm = 1; dst = OFF_WO + (size_t)(kb >> 2) * 163840 + (kb & 3) * 1024; sn = 4096; sk = 0; }
    else if ((u -= NU_O) < NU_GU) { const int m = u / 540, r = u % 540; src = m == 0 ? wg : wu; ld = FF; kb = r / 27; nb = r % 27; perm = 0;
        dst = OFF_WG + (size_t)m * 2211840 + (size_t)kb * 1024; sn = 20480; sk = 0; }
    else { u -= NU_GU; src = wd; ld = DM; kb = u / 10; nb = u % 10; perm = 1; dst = OFF_WD + (size_t)kb * 1024; sn = 54 * 1024; sk = 0; }
    (void)sk;
    LAS unsigned char* img = (LAS unsigned char*)lds + wave * (32 * 144);
    u32x4 t[4];
#pragma unroll
    for (int i = 0; i < 4; ++i) t[i] = *(const u32x4*)(src + (size_t)(kb * 32 + i * 8 + (lane >> 3)) * ld + nb * 64 + (lane & 7) * 8);
#pragma unroll
    for (int i = 0; i < 4; ++i) *(LAS u32x4*)(img + (i * 8 + (lane >> 3)) * 144 + (lane & 7) * 16) = t[i];
    asm volatile("s_waitcnt lgkmcnt(0)" ::: "memory");
    const int g = lane >> 4, qq = (lane >> 2) & 3, p = lane & 3;
    const int r0 = perm ? 4 * g + qq : 8 * g + qq, r1 = perm ? 16 + 4 * g + qq : 8 * g + 4 + qq;
#pragma unroll
    for (int i = 0; i < 4; ++i) {
        const u32x2 a = trd(img + r0 * 144 + i * 32 + p * 8), c = trd(img + r1 * 144 + i * 32 + p * 8);
        *(u32x4*)(base + dst + (size_t)(nb * 4 + i) * sn + lane * 16) = (u32x4){a.x, a.y, c.x, c.y};
    }
}

constexpr int L_MEM = 0, L_IMG = 10240;

__device__ __forceinline__ int kstep(int w, int p) { return p < 2 ? w + 4 * p : (p < 4 ? 6 + p : ((w + (p >> 1) - 1) & 3) + 4 * (p & 1)); }
#define GATHER_MEM()                                                                                                     \
    do {                                                                                                                 \
        u32x4 g2;                                                                                                        \
        u32 spins = cap;                                                                                                 \
        if (BELL2_NOPS >= 0 && s > 0) {                                                                                  \
            for (;;) {                                                                                                   \
                asm volatile("" ::: "memory");                                                                           \
                const u32 b_ = __builtin_amdgcn_raw_buffer_load_b32(RR, obell2, sel, AUX_SC1);                           \
                if (__builtin_amdgcn_ballot_w64((int)b_ < s) == 0) break;                                                \
                asm volatile("" : "+s"(spins));                                                                          \
                if (--spins == 0) break;                                                                                 \
            }                                                                                                            \
            STAMP(5);                                                                                                    \
            cap = spins ? cap : 1u;                                                                                      \
            spins = cap;                                                                                                 \
            _Pragma("unroll") for (int i_ = 0; i_ < BELL2_NOPS; ++i_) asm volatile("s_nop 7");                           \
            STAMP(6);                                                                                                    \
        }                                                                                                                \
        for (;;) {                                                                                                       \
            asm volatile("" ::: "memory");                                                                               \
            g2 = ld4(RR, mo2, slot); a[0] = ld4(RR, mo0, slot); a[1] = ld4(RR, mo1, slot);                               \
            const u32 t_ = mx4(mx4(mx4(0u, g2), a[0]), a[1]);                                                            \
            if (__builtin_amdgcn_ballot_w64(t_ == SENT) == 0) break;                                                     \
            asm volatile("" : "+s"(spins));                                                                              \
            if (--spins == 0) break;                                                                                     \
        }                                                                                                                \
        STAMP(0);                                                                                                        \
        STAMP_SET(7, cap - spins);                                                                                       \
        *(LAS u32x4*)(lw) = a[0];                                                                                        \
        *(LAS u32x4*)(lw + 4096) = a[1];                                                                                 \
        if (w < 2) *(LAS u32x4*)(lw + 8192) = g2;                                                                        \
        cap = spins ? cap : 1u;                                                                                          \
        SB();                                                                                                            \
    } while (0)
#define READ_MEM()                                                                                                       \
    do {                                                                                                                 \
        SB();                                                                                                            \
        BAR();                                                                                                           \
        STAMP(1);                                                                                                        \
        a[2] = *(LAS u32x4*)(l0 + 8192); SB(); a[3] = *(LAS u32x4*)(l0 + 9216); SB();                                    \
        a[4] = *(LAS u32x4*)(l1); SB(); a[5] = *(LAS u32x4*)(l1 + 4096); SB();                                           \
        a[6] = *(LAS u32x4*)(l2); SB(); a[7] = *(LAS u32x4*)(l2 + 4096); SB();                                           \
        a[8] = *(LAS u32x4*)(l3); SB(); a[9] = *(LAS u32x4*)(l3 + 4096); SB();                                           \
    } while (0)
#define CHAIN_SETUP()                                                                                                    \
    const int tid = threadIdx.x, lane = tid & 63, w = __builtin_amdgcn_readfirstlane(tid >> 6);                          \
    const int n = lane & 15, g = lane >> 4;                                                                              \
    GAS u32* flags = (GAS u32*)(base + OFF_FLAG);                                                                        \
    const u64 selm = sel_mask(flags, lane);                                                                              \
    const int sel = (int)(selm & 1) << 13;                                                                               \
    const rsrc_t RR = mk(base + OFF_RING, 4 * 65536);                                                                    \
    const u32 idx = (lane & 2) ? 0xEEEEu : 0x4444u;                                                                      \
    const u32x4 sent4 = {SENT, SENT, SENT, SENT};                                                                        \
    u32 cap = SPIN_CAP
#define MEM_SETUP()                                                                                                      \
    const int mo0 = xp(20480 + w * 1024 + lane * 16), mo1 = xp(20480 + (w + 4) * 1024 + lane * 16),                      \
              mo2 = w < 2 ? xp(20480 + (w + 8) * 1024 + lane * 16) : OOB;                                                \
    const int obell2 = xp(O_BELL + 512 + w * 128);                                                                       \
    LAS unsigned char* const l0 = L + L_MEM + lane * 16;                                                                 \
    LAS unsigned char* const lw = l0 + w * 1024;                                                                         \
    LAS unsigned char* const l1 = l0 + ((w + 1) & 3) * 1024;                                                             \
    LAS unsigned char* const l2 = l0 + ((w + 2) & 3) * 1024;                                                             \
    LAS unsigned char* const l3 = l0 + ((w + 3) & 3) * 1024
#define CLK0() do {} while (0)
#define CLK1() do {} while (0)

__device__ __forceinline__ void role_A(unsigned char* base, LAS unsigned char* L, const int h) {
    CHAIN_SETUP();
    MEM_SETUP();
    u32x8 wt[40];
    {
        const rsrc_t RW = mk(base + OFF_WQ, 2 * 5242880);
#pragma unroll
        for (int j = 0; j < 10; ++j)
#pragma unroll
            for (int t = 0; t < 4; ++t) {
                const int so = (t >> 1) * 5242880 + ((h * 8 + 2 * w + (t & 1)) * 20 + 2 * kstep(w, j)) * 1024;
                wt[j * 4 + t] = cat(ld4p(RW, lane * 16, so), ld4p(RW, lane * 16 + 1024, so));
            }
    }
    u32 knownK = 0;
    u32x4 kx[8];
    if (w == 0) {
        knownK = (u32)__builtin_amdgcn_readfirstlane((int)wait_ge(flags + F_K + h, 1u, cap, false));
        const rsrc_t RK = mk(base + OFF_KX + (size_t)h * 16384, 8192);
#pragma unroll
        for (int i = 0; i < 8; ++i) kx[i] = ld4(RK, lane * 16 + i * 1024);
    }
    const float ninf = g < 2 ? -INFINITY : 0.f;
    const f32x4 t0init = {ninf, ninf, ninf, ninf};
    const u32 ipa = L_IMG + (4 * g + (n >> 2)) * 32 + (n & 3) * 8;
    const int pox = xp(h * 512 + (g >> 1) * 256 + rmap(n & 7, g & 1) * 16 + (n >> 3) * 8);
    const int pom = g >= 2 ? xp(16384 + h * 128 + rmap(n & 7, n >> 3) * 8 + (g - 2) * 4) : OOB;
    LAS unsigned char* const iw = L + L_IMG + (32 * w + n) * 32 + 4 * g;
    const int pclr = lane < 40 ? xp(lane < 32 ? h * 512 + lane * 16 : 16384 + h * 128 + (lane - 32) * 16) : OOB;
    const int obell = (tid == 192 && (h & 7) == 0) ? xp(O_BELL + (h >> 3) * 128) : OOB;
    const float cfs = 0.08838834764831845f * 1.4426950408889634f;
    const f32x2 cf2 = {cfs, cfs};
    CLK0();
#pragma unroll 1
    for (int s = 0; s < NSEG; ++s) {
        const int slot = (s & 3) * 65536 + sel;
        STAMP_DECL;
        u32x4 a[10];
        f32x4 c0 = Z4, c1 = Z4, c2 = Z4, c3 = Z4;
        PRE4(c0, c1, c2, c3);
        GATHER_MEM();
#pragma unroll
        for (int j = 0; j < 10; ++j) {
            if (j == 2) READ_MEM();
            if (j == 3) { SB(); st1(RR, obell, sel, (u32)s + 1u); SB(); }
            if (j < 8) { SMF_A(c0, a[j], wt[j * 4], idx); SMF_A(c2, a[j], wt[j * 4 + 2], idx); SMF_A(c1, a[j], wt[j * 4 + 1], idx); SMF_A(c3, a[j], wt[j * 4 + 3], idx); }
            else { SMF_V(c0, a[j], wt[j * 4], idx); SMF_V(c2, a[j], wt[j * 4 + 2], idx); SMF_V(c1, a[j], wt[j * 4 + 1], idx); SMF_V(c3, a[j], wt[j * 4 + 3], idx); }
        }
        POST4(c0, c1, c2, c3);
        STAMP(2);
        *(LAS u32*)(iw) = pk2(fold(c0));
        *(LAS u32*)(iw + 16) = pk2(fold(c2));
        *(LAS u32*)(iw + 512) = pk2(fold(c1));
        *(LAS u32*)(iw + 528) = pk2(fold(c3));
        BAR();
        STAMP(3);
        if (w == 0) {
            if (s < NSEG - 1) {
                asm volatile(
                    "ds_read_b64_tr_b16 v[216:217], %[ip]\n\t"
                    "ds_read_b64_tr_b16 v[218:219], %[ip] offset:512\n\t"
                    "ds_read_b64_tr_b16 v[220:221], %[ip] offset:1024\n\t"
                    "ds_read_b64_tr_b16 v[222:223], %[ip] offset:1536\n\t"
                    "ds_read_b64_tr_b16 v[224:225], %[ip] offset:2048\n\t"
                    "ds_read_b64_tr_b16 v[226:227], %[ip] offset:2560\n\t"
                    "ds_read_b64_tr_b16 v[228:229], %[ip] offset:3072\n\t"
                    "ds_read_b64_tr_b16 v[230:231], %[ip] offset:3584\n\t"
                    "s_waitcnt lgkmcnt(6)\n\t"
                    "v_mfma_f32_16x16x32_bf16 v[240:243], v[216:219], v[216:219], %[ti]\n\t"
                    "v_mfma_f32_16x16x32_bf16 v[232:235], %[k0], v[216:219], 0\n\t"
                    "v_mfma_f32_16x16x32_bf16 v[236:239], %[k4], v[216:219], 0\n\t"
                    "s_waitcnt lgkmcnt(4)\n\t"
                    "v_mfma_f32_16x16x32_bf16 v[240:243], v[220:223], v[220:223], v[240:243]\n\t"
                    "v_mfma_f32_16x16x32_bf16 v[232:235], %[k1], v[220:223], v[232:235]\n\t"
                    "v_mfma_f32_16x16x32_bf16 v[236:239], %[k5], v[220:223], v[236:239]\n\t"
                    "s_waitcnt lgkmcnt(2)\n\t"
                    "v_mfma_f32_16x16x32_bf16 v[240:243], v[224:227], v[224:227], v[240:243]\n\t"
                    "v_mfma_f32_16x16x32_bf16 v[232:235], %[k2], v[224:227], v[232:235]\n\t"
                    "v_mfma_f32_16x16x32_bf16 v[236:239], %[k6], v[224:227], v[236:239]\n\t"
                    "s_waitcnt lgkmcnt(0)\n\t"
                    "v_mfma_f32_16x16x32_bf16 v[240:243], v[228:231], v[228:231], v[240:243]\n\t"
                    "v_mfma_f32_16x16x32_bf16 v[232:235], %[k3], v[228:231], v[232:235]\n\t"
                    "v_mfma_f32_16x16x32_bf16 v[236:239], %[k7], v[228:231], v[236:239]\n\t"
                    "s_nop 5\n\t"
                    "v_mov_b32_dpp v240, v242 row_ror:8 row_mask:0xf bank_mask:0xc\n\t"
                    "v_mov_b32_dpp v241, v243 row_ror:8 row_mask:0xf bank_mask:0xc\n\t"
                    "v_mov_b32_dpp v232, v236 row_ror:8 row_mask:0xf bank_mask:0xc\n\t"
                    "v_mov_b32_dpp v233, v237 row_ror:8 row_mask:0xf bank_mask:0xc\n\t"
                    "v_mov_b32_dpp v234, v238 row_ror:8 row_mask:0xf bank_mask:0xc\n\t"
                    "v_mov_b32_dpp v235, v239 row_ror:8 row_mask:0xf bank_mask:0xc\n\t"
                    "v_max3_f32 v244, v240, v241, v232\n\t"
                    "v_max3_f32 v244, v244, v233, v234\n\t"
                    "v_max_f32 v244, v244, v235\n\t"
                    "s_nop 1\n\t"
                    "v_max_f32_dpp v244, v244, v244 row_ror:8 row_mask:0xf bank_mask:0xf\n\t"
                    "v_mov_b32 v245, v244\n\t"
                    "s_nop 1\n\t"
                    "v_permlane16_swap_b32 v244, v245\n\t"
                    "v_max_f32 v244, v244, v245\n\t"
                    "v_mov_b32 v245, v244\n\t"
                    "s_nop 1\n\t"
                    "v_permlane32_swap_b32 v244, v245\n\t"
                    "v_max_f32 v244, v244, v245\n\t"
                    "v_pk_add_f32 v[232:233], v[232:233], v[244:245] op_sel_hi:[1,0] neg_lo:[0,1] neg_hi:[0,1]\n\t"
                    "v_pk_add_f32 v[234:235], v[234:235], v[244:245] op_sel_hi:[1,0] neg_lo:[0,1] neg_hi:[0,1]\n\t"
                    "v_pk_add_f32 v[240:241], v[240:241], v[244:245] op_sel_hi:[1,0] neg_lo:[0,1] neg_hi:[0,1]\n\t"
                    "v_pk_mul_f32 v[232:233], v[232:233], %[cf]\n\t"
                    "v_pk_mul_f32 v[234:235], v[234:235], %[cf]\n\t"
                    "v_pk_mul_f32 v[240:241], v[240:241], %[cf]\n\t"
                    "v_exp_f32 v232, v232\n\t"
                    "v_exp_f32 v233, v233\n\t"
                    "v_exp_f32 v234, v234\n\t"
                    "v_exp_f32 v235, v235\n\t"
                    "v_exp_f32 v240, v240\n\t"
                    "v_exp_f32 v241, v241\n\t"
                    "v_pk_add_f32 v[244:245], v[232:233], v[234:235]\n\t"
                    "v_pk_add_f32 v[244:245], v[244:245], v[240:241]\n\t"
                    "v_add_f32 v244, v244, v245\n\t"
                    "s_nop 1\n\t"
                    "v_add_f32_dpp v244, v244, v244 row_ror:8 row_mask:0xf bank_mask:0xf\n\t"
                    "v_mov_b32 v245, v244\n\t"
                    "s_nop 1\n\t"
                    "v_permlane16_swap_b32 v244, v245\n\t"
                    "v_add_f32 v244, v244, v245\n\t"
                    "v_mov_b32 v245, v244\n\t"
                    "s_nop 1\n\t"
                    "v_permlane32_swap_b32 v244, v245\n\t"
                    "v_add_f32 v244, v244, v245\n\t"
                    "v_rcp_f32 v244, v244\n\t"
                    "s_nop 0\n\t"
                    "v_pk_mul_f32 v[232:233], v[232:233], v[244:245] op_sel_hi:[1,0]\n\t"
                    "v_pk_mul_f32 v[234:235], v[234:235], v[244:245] op_sel_hi:[1,0]\n\t"
                    "v_pk_mul_f32 v[240:241], v[240:241], v[244:245] op_sel_hi:[1,0]\n\t"
                    "v_cvt_pk_bf16_f32 v232, v232, v233\n\t"
                    "v_cvt_pk_bf16_f32 v233, v234, v235\n\t"
                    "buffer_store_dwordx2 v[232:233], %[px], %[rr], %[so] offen sc1\n\t"
                    "v_cvt_pk_bf16_f32 v240, v240, v241\n\t"
                    "buffer_store_dword v240, %[pm], %[rr], %[so] offen sc1\n\t"
                    :
                    : [ip] "v"(ipa), [ti] "v"(t0init), [k0] "v"(kx[0]), [k1] "v"(kx[1]), [k2] "v"(kx[2]), [k3] "v"(kx[3]), [k4] "v"(kx[4]),
                      [k5] "v"(kx[5]), [k6] "v"(kx[6]), [k7] "v"(kx[7]), [cf] "v"(cf2), [px] "v"(pox), [pm] "v"(pom), [rr] "s"(RR), [so] "s"(slot)
                    : "memory", "v216", "v217", "v218", "v219", "v220", "v221", "v222", "v223", "v224", "v225", "v226", "v227", "v228", "v229", "v230",
                      "v231", "v232", "v233", "v234", "v235", "v236", "v237", "v238", "v239", "v240", "v241", "v242", "v243", "v244", "v245");
                STAMPW(4);
                st4(RR, pclr, ((s + 2) & 3) * 65536 + sel, sent4);
                if (knownK <= (u32)s + 1u) knownK = (u32)__builtin_amdgcn_readfirstlane((int)wait_ge(flags + F_K + h, (u32)s + 2u, cap, false));
                const rsrc_t RK = mk(base + OFF_KX + ((size_t)(s + 1) * NH + h) * 16384, 8192);
#pragma unroll
                for (int i = 0; i < 8; ++i) kx[i] = ld4(RK, lane * 16 + i * 1024);
            }
        } else if (w == 1) {
            const rsrc_t RKM = mk(base + OFF_KM + ((size_t)s * NH + h) * 2048, 2048);
#pragma unroll
            for (int e = 0; e < 4; ++e) {
                const u32x2 lo = trd(L + ipa + e * 1024), hi = trd(L + ipa + e * 1024 + 512);
                st4(RKM, n >= 8 ? e * 512 + (g * 8 + n - 8) * 16 : OOB, 0, (u32x4){lo.x, lo.y, hi.x, hi.y});
            }
            VMCNT(0);
            if (lane == 0) __hip_atomic_store(flags + F_KM + h * 32, (u32)s + 1u, RLX_AGENT);
        }
        STAMP_FLUSH(s);
    }
    CLK1();
    if (cap == 1u && lane == 0) __hip_atomic_store(flags + F_TMO, 1u, RLX_AGENT);
}

__device__ __forceinline__ void role_B(unsigned char* base, LAS unsigned char* L, const int h) {
    CHAIN_SETUP();
    MEM_SETUP();
    u32x8 wt[40];
    {
        const rsrc_t RW = mk(base + OFF_WV, 2 * 5242880);
#pragma unroll
        for (int j = 0; j < 10; ++j)
#pragma unroll
            for (int t = 0; t < 2; ++t) {
                const int so = ((h * 8 + 2 * w + t) * 20 + 2 * kstep(w, j)) * 1024;
                wt[j * 2 + t] = cat(ld4p(RW, lane * 16, so), ld4p(RW, lane * 16 + 1024, so));
            }
#pragma unroll
        for (int i = 0; i < 10; ++i)
#pragma unroll
            for (int t = 0; t < 2; ++t) {
                const int so = 5242880 + h * 163840 + (10 * w + i) * 4096 + t * 2048;
                wt[20 + 2 * i + t] = cat(ld4p(RW, lane * 16, so), ld4p(RW, lane * 16 + 1024, so));
            }
    }
    LAS unsigned char* const ip = L + L_IMG + (16 * (g >> 1) + 4 * (g & 1) + ((n >> 2) & 1) + 8 * (n >> 3)) * 32 + 8 * (n & 3);
    LAS unsigned char* const iw = L + L_IMG + (16 * w + 2 * (n >> 2) + (n & 1)) * 32 + 8 * g + 2 * (n & 2);
    const int mso = (10 * w + (g & 1)) * 16384 + h * 256 + n * 16 + (g >> 1) * 8;
    const int mcl = (10 * w + g) * 16384 + h * 256 + n * 16, mcl2 = g < 2 ? mcl : OOB;
    CLK0();
#pragma unroll 1
    for (int s = 0; s < NSEG; ++s) {
        const int slot = (s & 3) * 65536 + sel;
        STAMP_DECL;
        const rsrc_t RM = mk(m_base(base, s, selm), 655360);
        u32x4 a[10];
        f32x4 c0 = Z4, c1 = Z4, c[10];
#pragma unroll
        for (int i = 0; i < 10; ++i) c[i] = Z4;
        PRE4(c[0], c[1], c[2], c[3]); PRE4(c[4], c[5], c[6], c[7]); PRE4(c[8], c[9], c0, c1);
        GATHER_MEM();
#pragma unroll
        for (int j = 0; j < 10; ++j) {
            if (j == 2) READ_MEM();
            SMF_A(c0, a[j], wt[j * 2], idx); SMF_A(c1, a[j], wt[j * 2 + 1], idx);
        }
        POST2(c0, c1);
        STAMP(2);
        *(LAS u32*)(iw) = pk2(fold(c0));
        *(LAS u32*)(iw + 256) = pk2(fold(c1));
        BAR();
        STAMP(3);
        u32x4 av[2];
#pragma unroll
        for (int t = 0; t < 2; ++t) { const u32x2 lo = trd(ip + t * 1024), hi = trd(ip + t * 1024 + 64); av[t] = (u32x4){lo.x, lo.y, hi.x, hi.y}; }
#define B_MF(i, t) do { if ((i) < 6) SMF_A(c[i], av[t], wt[20 + 2 * (i) + (t)], idx); else SMF_V(c[i], av[t], wt[20 + 2 * (i) + (t)], idx); SB(); } while (0)
#define B_E1(i) do { asm volatile("v_pk_add_f32 %0, %2, %3\n\tv_pk_add_f32 %1, %4, %5" : "=&v"(f0), "=&v"(f1) : "v"(__builtin_shufflevector(c[i], c[i], 0, 1)), \
            "v"(__builtin_shufflevector(c[i], c[i], 2, 3)), "v"(__builtin_shufflevector(c[(i) + 1], c[(i) + 1], 0, 1)), "v"(__builtin_shufflevector(c[(i) + 1], c[(i) + 1], 2, 3))); SB(); } while (0)
#define B_E2() do { asm volatile("v_cvt_pk_bf16_f32 v216, %0, %1\n\tv_cvt_pk_bf16_f32 v217, %2, %3" :: "v"(f0.x), "v"(f0.y), "v"(f1.x), "v"(f1.y) : "v216", "v217"); SB(); } while (0)
#define B_E3(i, pad) do { asm volatile(pad "v_permlane16_swap_b32 v216, v217\n\tbuffer_store_dwordx2 v[216:217], %0, %1, %2 offen sc1" :: "v"(mso), "s"(RM), "s"((i) * 16384) : "memory", "v216", "v217"); SB(); } while (0)
        f32x2 f0, f1;
        B_MF(0, 0); B_MF(1, 0); B_MF(0, 1); B_MF(1, 1);
        B_MF(2, 0); B_MF(3, 0); B_MF(2, 1); B_MF(3, 1);
#pragma unroll
        for (int i = 4; i < 10; i += 2) {
            B_MF(i, 0); B_E1(i - 4); B_MF(i + 1, 0); B_E2(); B_MF(i, 1); B_E3(i - 4, "s_nop 0\n\t"); B_MF(i + 1, 1);
        }
        B_E1(6); B_E2(); B_E3(6, "s_nop 1\n\t");
        B_E1(8); B_E2(); B_E3(8, "s_nop 1\n\t");
        STAMPW(4);
        if (s > 0 && tid == 0) __hip_atomic_store(flags + F_M + (s - 1) * 128 + (h >> 3) * 32 + (h & 7), 1u, RLX_AGENT);
        if (s + 2 < NSEG) {
            const rsrc_t RM2 = mk(m_base(base, s + 2, selm), 655360);
#pragma unroll
            for (int i = 0; i < 3; ++i) st4(RM2, i < 2 ? mcl : mcl2, i * 65536, sent4);
        }
        STAMP_FLUSH(s);
    }
    CLK1();
    VMCNT(0);
    BAR();
    if (tid == 0) __hip_atomic_store(flags + F_M + (NSEG - 1) * 128 + (h >> 3) * 32 + (h & 7), 1u, RLX_AGENT);
    if (cap == 1u && lane == 0) __hip_atomic_store(flags + F_TMO, 1u, RLX_AGENT);
}

__device__ __forceinline__ void role_C(unsigned char* base, LAS unsigned char* L, const int cs) {
    CHAIN_SETUP();
    const int mo = cs * 16384 + w * 2048 + lane * 16, po = xp(w * 4096 + lane * 16), pmo = xp(16384 + (8 * w + 2 * g) * 128 + n * 8);
    LAS float* red = (LAS float*)L + w * 128 + 2 * g * 16 + n;
    LAS unsigned char* rp = L + ((2 * g + ((lane >> 2) & 1)) * 16 + 4 * (lane & 3) + ((lane >> 2) & 2)) * 4;
    const int opub = xp(20480 + cs * 256 + lane * 4), oclr = lane < 16 ? xp(20480 + cs * 256 + lane * 16) : OOB;
    const int obell = xp(O_BELL + w * 128), oring = (tid == 192 && cs % 10 == 0) ? xp(O_BELL + 512 + (cs / 10) * 128) : OOB;
    u32 knownU = 0, knownU1 = 0;
#pragma unroll 1
    for (int s = 0; s < NSEG - 1; ++s) {
        const int slot = (s & 3) * 65536 + sel;
        STAMP_DECL;
        if (knownU <= (u32)s) knownU = wave_min(wait_ge(flags + F_U + 32 * (s & 1) + 8 * w + (lane & 7), (u32)s + 1u, cap, false));
        { const u32 t_ = knownU; knownU = knownU1; knownU1 = t_; }
        u32x8 uf[4];
        {
            const rsrc_t RU = mk(base + OFF_U + (size_t)s * 1310720 + (size_t)cs * 32768 + w * 8192, 8192);
#pragma unroll
            for (int t = 0; t < 4; ++t) uf[t] = cat(ld4(RU, lane * 16 + t * 2048), ld4(RU, lane * 16 + t * 2048 + 1024));
        }
        const rsrc_t RM = mk(m_base(base, s, selm), 655360);
        f32x4 c0 = Z4, c1 = Z4;
        PRE2(c0, c1);
        u32x4 m0, m1, p0, p1, p2, p3; u32x2 pa, pb;
        u32 spins = cap;
        for (;;) {
            asm volatile("" ::: "memory");
            const u32 b_ = __builtin_amdgcn_raw_buffer_load_b32(RR, obell, sel, AUX_SC1);
            if (__builtin_amdgcn_ballot_w64((int)b_ <= s) == 0) break;
            asm volatile("" : "+s"(spins));
            if (--spins == 0) break;
        }
        STAMP(4);
        cap = spins ? cap : 1u;
        spins = cap;
#pragma unroll
        for (int i = 0; i < BELL_NOPS - BELL_M; ++i) asm volatile("s_nop 7");
        asm volatile("" ::: "memory");
        m0 = ld4(RM, mo); m1 = ld4(RM, mo + 1024);
        SB();
#pragma unroll
        for (int i = 0; i < BELL_M; ++i) asm volatile("s_nop 7");
        SB();
        STAMP(5);
        p0 = ld4(RR, po, slot); p1 = ld4(RR, po + 1024, slot); p2 = ld4(RR, po + 2048, slot); p3 = ld4(RR, po + 3072, slot);
        pa = ld2(RR, pmo, slot); pb = ld2(RR, pmo + 128, slot);
        SB();
        st1(RR, oring, sel, (u32)s + 1u);
        SB();
        for (;;) {
            const u32 t_ = mx3(mx3(mx4(mx4(mx4(mx4(mx4(mx4(0u, m0), m1), p0), p1), p2), p3), pa.x, pa.y), pb.x, pb.y);
            if (__builtin_amdgcn_ballot_w64(t_ == SENT) == 0) break;
            asm volatile("" : "+s"(spins) :: "memory");
            if (--spins == 0) break;
            m0 = ld4(RM, mo); m1 = ld4(RM, mo + 1024);
            p0 = ld4(RR, po, slot); p1 = ld4(RR, po + 1024, slot); p2 = ld4(RR, po + 2048, slot); p3 = ld4(RR, po + 3072, slot);
            pa = ld2(RR, pmo, slot); pb = ld2(RR, pmo + 128, slot);
        }
        STAMP_SET(3, cap - spins);
        STAMP(6);
        const u32x8 mf = cat(m0, m1);
        const u32x4 pm = {pa.x, pa.y, pb.x, pb.y};
        SMF_V(c0, p0, uf[0], idx); SMF_V(c1, p1, uf[1], idx); SMF_V(c0, p2, uf[2], idx); SMF_V(c1, p3, uf[3], idx); SMF_V(c0, pm, mf, idx);
        POST2(c0, c1);
        const f32x2 f = fold(c0) + fold(c1);
        red[0] = f.x;
        red[16] = f.y;
        BAR();
        if (w == 0) {
            const f32x2 r0 = *(LAS f32x2*)rp, r1 = *(LAS f32x2*)(rp + 512), r2 = *(LAS f32x2*)(rp + 1024), r3 = *(LAS f32x2*)(rp + 1536);
            const f32x2 r = (r0 + r1) + (r2 + r3);
            st1(RR, opub, ((s + 1) & 3) * 65536 + sel, pk2(r));
            STAMPW(7);
            st4(RR, oclr, ((s + 3) & 3) * 65536 + sel, sent4);
        }
        BAR();
        cap = spins ? cap : 1u;
        STAMP_FLUSH(s);
    }
    if (cap == 1u && lane == 0) __hip_atomic_store(flags + F_TMO, 1u, RLX_AGENT);
}

constexpr float MASKED = -1.0e9f / 0.08838834764831845f;
constexpr int NTG = 2, NOS = 5;
constexpr int L_XF = 0, L_EX = 81920;

template <int I> __device__ __forceinline__ void dma1(const unsigned char* sbase, u32 vo, u32 dst) {
    if constexpr ((I & 3) == 0)
        asm volatile("s_mov_b32 m0, %2\n\ts_nop 0\n\tglobal_load_lds_dwordx4 %0, %1" :: "v"(vo), "s"(sbase + I * 1024), "s"(dst + I * 1024) : "memory");
    else
        asm volatile("global_load_lds_dwordx4 %0, %1 offset:%2" :: "v"(vo), "s"(sbase + (I & ~3) * 1024), "i"((I & 3) * 1024) : "memory");
}
__device__ __forceinline__ void dma_i(int i, const unsigned char* sbase, u32 vo, u32 dst) {
    switch (i) {
        case 0: dma1<0>(sbase, vo, dst); break; case 1: dma1<1>(sbase, vo, dst); break; case 2: dma1<2>(sbase, vo, dst); break;
        case 3: dma1<3>(sbase, vo, dst); break; case 4: dma1<4>(sbase, vo, dst); break; case 5: dma1<5>(sbase, vo, dst); break;
        case 6: dma1<6>(sbase, vo, dst); break; case 7: dma1<7>(sbase, vo, dst); break; case 8: dma1<8>(sbase, vo, dst); break;
        default: dma1<9>(sbase, vo, dst); break;
    }
}
__device__ __forceinline__ void dma_xf(const unsigned char* base, int seg, int buf, int w, int lane) {
#pragma unroll
    for (int i = 0; i < 10; ++i) dma_i(i, base + OFF_XF + (size_t)seg * 40960 + w * 10240, lane * 16, L_XF + buf * 40960 + w * 10240);
}
#define PROJ(NT, acc, WT, O, xb, nsrc, ndst)                                                                             \
    do {                                                                                                                 \
        u32x4 bq[3][2];                                                                                                  \
        _Pragma("unroll") for (int i_ = 0; i_ < 2; ++i_) {                                                               \
            bq[i_][0] = *(LAS u32x4*)((xb) + i_ * 2048); bq[i_][1] = *(LAS u32x4*)((xb) + i_ * 2048 + 1024);             \
        }                                                                                                                \
        SB();                                                                                                            \
        _Pragma("unroll") for (int ks = 0; ks < 20; ++ks) {                                                              \
            if (ks + 2 < 20) {                                                                                           \
                bq[(ks + 2) % 3][0] = *(LAS u32x4*)((xb) + (ks + 2) * 2048);                                             \
                bq[(ks + 2) % 3][1] = *(LAS u32x4*)((xb) + (ks + 2) * 2048 + 1024);                                      \
            }                                                                                                            \
            SB();                                                                                                        \
            _Pragma("unroll") for (int tt = 0; tt < 2; ++tt) _Pragma("unroll") for (int t = 0; t < (NT); ++t) {          \
                if (ks == 0) {                                                                                           \
                    if ((O) + t < 64) MF0_AV(acc[tt * (NT) + t], WT[(O) + t], bq[0][tt]);                                \
                    else MF0_VV(acc[tt * (NT) + t], WT[(O) + t], bq[0][tt]);                                             \
                } else if ((O) + (NT) * ks + t < 64) MF_AV(acc[tt * (NT) + t], WT[(O) + (NT) * ks + t], bq[ks % 3][tt]); \
                else MF_VV(acc[tt * (NT) + t], WT[(O) + (NT) * ks + t], bq[ks % 3][tt]);                                 \
            }                                                                                                            \
            SB();                                                                                                        \
            if ((ks & 1) == 0) dma_i(ks >> 1, (nsrc), lane * 16, (ndst));                                                \
            SB();                                                                                                        \
        }                                                                                                                \
    } while (0)
__device__ __forceinline__ u32x4 pk8(f32x4 a, f32x4 b) { return (u32x4){pk(a[0], a[1]), pk(a[2], a[3]), pk(b[0], b[1]), pk(b[2], b[3])}; }
__device__ __forceinline__ u32x4 keyfrag(u32 a01, u32 a23, u32 b01, u32 b23) {
    auto x = __builtin_amdgcn_permlane16_swap(a01, b01, false, false);
    auto y = __builtin_amdgcn_permlane16_swap(a23, b23, false, false);
    return (u32x4){x[0], x[1], y[0], y[1]};
}
#define STREAM_SETUP()                                                                                                   \
    const int tid = threadIdx.x, lane = tid & 63, w = __builtin_amdgcn_readfirstlane(tid >> 6);                          \
    GAS u32* flags = (GAS u32*)(base + OFF_FLAG);                                                                        \
    const unsigned char* const xsrc = base + OFF_XF + w * 10240

__device__ __forceinline__ void role_D(unsigned char* base, LAS unsigned char* L, const int h, const int par) {
    STREAM_SETUP();
    dma_xf(base, par, 0, w, lane);
    u32x4 wt[80];
    {
        const rsrc_t RW = mk(base + OFF_WV, 2 * 5242880);
#pragma unroll
        for (int ks = 0; ks < 20; ++ks)
#pragma unroll
            for (int t = 0; t < 2; ++t) wt[2 * ks + t] = ld4p(RW, lane * 16, ((h * 8 + 2 * w + t) * 20 + ks) * 1024);
#pragma unroll
        for (int i = 0; i < 40; ++i) wt[40 + i] = ld4p(RW, lane * 16, 5242880 + h * 163840 + w * 40960 + i * 1024);
    }
    VMCNT(0);
    BAR();
    MARK(1);
#pragma unroll 1
    for (int s = par; s < NSEG; s += 2) {
        STAMP_DECL;
        STAMPW(0);
        f32x4 acc[4];
        LAS unsigned char* xb = L + L_XF + ((s >> 1) & 1) * 40960 + lane * 16;
        PROJ(2, acc, wt, 0, xb, xsrc + (size_t)((s + 2) & (NSEG - 1)) * 40960, L_XF + (((s >> 1) + 1) & 1) * 40960 + w * 10240);
        POST4(acc[0], acc[1], acc[2], acc[3]);
        STAMPW(1);
        *(LAS u32x4*)(L + L_EX + w * 1024 + lane * 16) = pk8(acc[0], acc[1]);
        *(LAS u32x4*)(L + L_EX + 4096 + w * 1024 + lane * 16) = pk8(acc[2], acc[3]);
        BAR();
        STAMPW(2);
        u32x4 vf[8];
#pragma unroll
        for (int i = 0; i < 4; ++i) { vf[i] = *(LAS u32x4*)(L + L_EX + i * 1024 + lane * 16); vf[4 + i] = *(LAS u32x4*)(L + L_EX + 4096 + i * 1024 + lane * 16); SB(); }
        const rsrc_t RU = mk(base + OFF_U + (size_t)s * 1310720, 1310720);
        f32x4 d[10][2];
        u32 e0 = 0, e1 = 0, e2 = 0, e3 = 0;
#pragma unroll
        for (int i = 0; i < 12; ++i) {
            if (i == 10) { SB(); asm volatile("s_nop 7"); }
#pragma unroll
            for (int e = 0; e < 4; ++e) {
                if (i < 10) {
                    if (e == 0) {
                        if (40 + 4 * i < 64) { MF0_VA(d[i][0], vf[0], wt[40 + 4 * i]); MF0_VA(d[i][1], vf[4], wt[40 + 4 * i]); }
                        else { MF0_VV(d[i][0], vf[0], wt[40 + 4 * i]); MF0_VV(d[i][1], vf[4], wt[40 + 4 * i]); }
                    } else if (40 + 4 * i + e < 64) { MF_VA(d[i][0], vf[e], wt[40 + 4 * i + e]); MF_VA(d[i][1], vf[4 + e], wt[40 + 4 * i + e]); }
                    else { MF_VV(d[i][0], vf[e], wt[40 + 4 * i + e]); MF_VV(d[i][1], vf[4 + e], wt[40 + 4 * i + e]); }
                }
                SB();
                if (i >= 2) {
                    if (e == 0) { e0 = pk(d[i - 2][0][0], d[i - 2][0][1]); e1 = pk(d[i - 2][0][2], d[i - 2][0][3]); }
                    if (e == 1) { e2 = pk(d[i - 2][1][0], d[i - 2][1][1]); e3 = pk(d[i - 2][1][2], d[i - 2][1][3]); }
                    if (e == 2) st4(RU, lane * 16, (10 * w + i - 2) * 32768 + h * 1024, keyfrag(e0, e1, e2, e3));
                }
                SB();
            }
        }
        STAMPW(3);
        if (s < 4) VMCNT(0); else VMCNT(10);
        STAMPW(4);
        BAR();
        if (tid == 0) __hip_atomic_store(flags + F_U + 32 * par + h, (u32)(s < 4 ? s + 1 : s - 1), RLX_AGENT);
        STAMP_FLUSH(s);
    }
    VMCNT(0);
    BAR();
    if (tid == 0) __hip_atomic_store(flags + F_U + 32 * par + h, (u32)NSEG, RLX_AGENT);
}

__device__ __forceinline__ void role_E(unsigned char* base, LAS unsigned char* L, const int h) {
    STREAM_SETUP();
    dma_xf(base, 0, 0, w, lane);
    u32x4 wt[80];
    {
        const rsrc_t RW = mk(base + OFF_WQ, 2 * 5242880);
#pragma unroll
        for (int ks = 0; ks < 20; ++ks)
#pragma unroll
            for (int t = 0; t < 4; ++t) wt[4 * ks + t] = ld4p(RW, lane * 16, (t < 2 ? 5242880 : 0) + ((h * 8 + 2 * w + (t & 1)) * 20 + ks) * 1024);
    }
    VMCNT(0);
    BAR();
    MARK(1);
#pragma unroll 1
    for (int s = 0; s < NSEG; ++s) {
        STAMP_DECL;
        STAMPW(0);
        f32x4 acc[8];
        LAS unsigned char* xb = L + L_XF + (s & 1) * 40960 + lane * 16;
        PROJ(4, acc, wt, 0, xb, xsrc + (size_t)((s + 1) & (NSEG - 1)) * 40960, L_XF + ((s + 1) & 1) * 40960 + w * 10240);
        asm volatile("" : "+v"(acc[0]), "+v"(acc[1]), "+v"(acc[2]), "+v"(acc[3]));
        POST4(acc[4], acc[5], acc[6], acc[7]);
        STAMPW(1);
        const rsrc_t RK = mk(base + OFF_KX + ((size_t)s * NH + h) * 16384, 16384);
        st4(RK, lane * 16, w * 1024, pk8(acc[0], acc[1]));
        st4(RK, lane * 16, 4096 + w * 1024, pk8(acc[4], acc[5]));
        st4(RK, lane * 16, 8192 + w * 1024, pk8(acc[2], acc[3]));
        st4(RK, lane * 16, 12288 + w * 1024, pk8(acc[6], acc[7]));
        STAMPW(2);
        if (s < 6) VMCNT(0); else VMCNT(4);
        STAMPW(3);
        BAR();
        if (tid == 0) __hip_atomic_store(flags + F_K + h, (u32)(s < 6 ? s + 1 : s), RLX_AGENT);
        STAMPW(4);
        STAMP_FLUSH(s);
    }
    VMCNT(0);
    BAR();
    if (tid == 0) __hip_atomic_store(flags + F_K + h, (u32)NSEG, RLX_AGENT);
}

__device__ __forceinline__ void role_P(unsigned char* base, const int i) {
    const int tid = threadIdx.x, lane = tid & 63, w = __builtin_amdgcn_readfirstlane(tid >> 6);
    const int n = lane & 15, g = lane >> 4, h = i * 2 + (w >> 1), t = w & 1;
    GAS u32* flags = (GAS u32*)(base + OFF_FLAG);
    u32 cap = SPIN_CAP, knownK, knownM = 0;
    const int kmo = n >= 8 ? (g * 8 + n - 8) * 16 : OOB, tok = 16 * t + n;
    u32x4 kx[8], qf[4], km[4];
    knownK = (u32)__builtin_amdgcn_readfirstlane((int)wait_ge(flags + F_K + h, 1u, cap, true));
    {
        const rsrc_t RK = mk(base + OFF_KX + (size_t)h * 16384, 16384);
#pragma unroll
        for (int e = 0; e < 4; ++e) { kx[e] = ld4(RK, lane * 16, e * 1024); kx[4 + e] = ld4(RK, lane * 16, 4096 + e * 1024); qf[e] = ld4(RK, lane * 16, 8192 + t * 4096 + e * 1024); }
    }
#pragma unroll 1
    for (int s = 0; s < NSEG; ++s) {
        STAMP_DECL;
        STAMPW(0);
        const rsrc_t RKM = mk(base + OFF_KM + ((size_t)s * NH + h) * 2048, 2048);
        const bool early = knownM > (u32)s;
        if (early) {
#pragma unroll
            for (int e = 0; e < 4; ++e) km[e] = ld4(RKM, kmo, e * 512);
        }
        f32x4 t0, t1, t2;
        MF0_VV(t1, kx[0], qf[0]); MF0_VV(t2, kx[4], qf[0]);
#pragma unroll
        for (int e = 1; e < 4; ++e) { MF_VV(t1, kx[e], qf[e]); MF_VV(t2, kx[4 + e], qf[e]); }
        POST2(t1, t2);
        float y[12];
#pragma unroll
        for (int r = 0; r < 4; ++r) {
            y[r] = 4 * g + r <= tok ? t1[r] : MASKED;
            y[4 + r] = 16 + 4 * g + r <= tok ? t2[r] : MASKED;
        }
        const float mxx = fmax3(fmax3(y[0], y[1], y[2]), fmax3(y[3], y[4], y[5]), fmax3(y[6], y[7], y[7]));
        STAMPW(1);
        if (!early) {
            knownM = (u32)__builtin_amdgcn_readfirstlane((int)wait_ge(flags + F_KM + h * 32, (u32)s + 1u, cap, false));
#pragma unroll
            for (int e = 0; e < 4; ++e) km[e] = ld4(RKM, kmo, e * 512);
        }
        STAMPW(2);
        MF0_VV(t0, km[0], qf[0]);
#pragma unroll
        for (int e = 1; e < 4; ++e) MF_VV(t0, km[e], qf[e]);
        asm volatile("s_nop 7" : "+v"(t0));
#pragma unroll
        for (int r = 0; r < 4; ++r) y[8 + r] = g >= 2 ? t0[r] : -INFINITY;
        float mx = fmax3(fmax3(mxx, y[8], y[9]), y[10], y[11]);
        mx = max16(mx); mx = max32(mx);
        const float cf = 0.08838834764831845f * 1.4426950408889634f;
        float sum = 0.f;
#pragma unroll
        for (int r = 0; r < 12; ++r) { y[r] = __builtin_amdgcn_exp2f((y[r] - mx) * cf); sum += y[r]; }
        sum = add16(sum); sum = add32(sum);
        const float rs = __builtin_amdgcn_rcpf(sum);
        const rsrc_t RP = mk(base + OFF_PX + (size_t)s * 65536 + t * 32768 + h * 1024, 1024);
        st4(RP, lane * 16, 0, keyfrag(pk(y[0] * rs, y[1] * rs), pk(y[2] * rs, y[3] * rs), pk(y[4] * rs, y[5] * rs), pk(y[6] * rs, y[7] * rs)));
        const rsrc_t RQ = mk(base + OFF_PM + (size_t)s * 16384 + t * 8192 + h * 256, 256);
        st2(RQ, g >= 2 ? n * 16 + (g - 2) * 8 : OOB, 0, (u32x2){pk(y[8] * rs, y[9] * rs), pk(y[10] * rs, y[11] * rs)});
        STAMPW(3);
        if (s + 1 < NSEG) {
            if (knownK <= (u32)s + 1u) knownK = (u32)__builtin_amdgcn_readfirstlane((int)wait_ge(flags + F_K + h, (u32)s + 2u, cap, true));
            const rsrc_t RK = mk(base + OFF_KX + ((size_t)(s + 1) * NH + h) * 16384, 16384);
#pragma unroll
            for (int e = 0; e < 4; ++e) { kx[e] = ld4(RK, lane * 16, e * 1024); kx[4 + e] = ld4(RK, lane * 16, 4096 + e * 1024); qf[e] = ld4(RK, lane * 16, 8192 + t * 4096 + e * 1024); }
            u32 pm_;
            asm volatile("global_load_dword %0, %1, off sc1" : "=v"(pm_) : "v"(flags + F_KM + h * 32) : "memory");
            VMCNT(13);
            if (lane == 0) __hip_atomic_store(flags + F_PX + s * 128 + (h >> 3) * 32 + 8 * t + (h & 7), 1u, RLX_AGENT);
            asm volatile("s_waitcnt vmcnt(1)" : "+v"(pm_) :: "memory");
            knownM = (u32)__builtin_amdgcn_readfirstlane((int)pm_);
            __builtin_amdgcn_fence(__ATOMIC_ACQUIRE, "wavefront");
        } else {
            VMCNT(0);
            if (lane == 0) __hip_atomic_store(flags + F_PX + s * 128 + (h >> 3) * 32 + 8 * t + (h & 7), 1u, RLX_AGENT);
        }
        STAMPW(4);
        STAMP_FLUSH(s);
    }
    if (cap == 1u && lane == 0) __hip_atomic_store(flags + F_TMO, 1u, RLX_AGENT);
}

__device__ __forceinline__ void role_G(unsigned char* base, LAS unsigned char* L, const int i) {
    STREAM_SETUP();
    const int set = i % 14, tg = i / 14;
    const int fs = 4 * set + w, fsc = fs < 54 ? fs : 53;
    dma_xf(base, tg, 0, w, lane);
    u32x4 wt[80];
    {
        const rsrc_t RW = mk(base + OFF_WG, 2 * 2211840);
#pragma unroll
        for (int ks = 0; ks < 20; ++ks)
#pragma unroll
            for (int t = 0; t < 4; ++t) wt[4 * ks + t] = ld4p(RW, lane * 16, (t >> 1) * 2211840 + ((2 * fsc + (t & 1)) * 20 + ks) * 1024);
    }
    VMCNT(0);
    BAR();
    MARK(1);
    int it = 0;
#pragma unroll 1
    for (int s = tg; s < NSEG; s += NTG, ++it) {
        f32x4 acc[8];
        LAS unsigned char* xb = L + L_XF + (it & 1) * 40960 + lane * 16;
        PROJ(4, acc, wt, 0, xb, xsrc + (size_t)(s + NTG < NSEG ? s + NTG : 0) * 40960, L_XF + ((it + 1) & 1) * 40960 + w * 10240);
        asm volatile("" : "+v"(acc[0]), "+v"(acc[1]), "+v"(acc[2]), "+v"(acc[3]));
        POST4(acc[4], acc[5], acc[6], acc[7]);
#pragma unroll
        for (int q = 0; q < 4; ++q)
#pragma unroll
            for (int r = 0; r < 4; ++r) {
                f32x4& ga = acc[(q >> 1) * 4 + (q & 1)];
                const float gt = rbf(ga[r]);
                ga[r] = (gt * (gt > 0.f ? 1.f : 0.f)) * rbf(acc[(q >> 1) * 4 + (q & 1) + 2][r]);
            }
        const rsrc_t RH = mk(base + OFF_HF + (size_t)s * 110592, 110592);
        st4(RH, fs < 54 ? lane * 16 : OOB, fsc * 1024, pk8(acc[0], acc[1]));
        st4(RH, fs < 54 ? lane * 16 : OOB, 55296 + fsc * 1024, pk8(acc[4], acc[5]));
        VMCNT(2);
        BAR();
        if (tid == 0 && it > 0) __hip_atomic_fetch_add(flags + F_H + s - NTG, 1u, RLX_AGENT);
    }
    VMCNT(0);
    BAR();
    if (tid == 0) __hip_atomic_fetch_add(flags + F_H + tg + NTG * (it - 1), 1u, RLX_AGENT);
    MARK(2);
}

constexpr int L_RD = 0, L_BASE = 16384;
__device__ __forceinline__ void role_O(unsigned char* base, LAS unsigned char* L, const int i, const u32* __restrict__ x, u32* __restrict__ out) {
    const int tid = threadIdx.x, lane = tid & 63, w = __builtin_amdgcn_readfirstlane(tid >> 6);
    const int n = lane & 15, g = lane >> 4;
    GAS u32* flags = (GAS u32*)(base + OFF_FLAG);
    u32 cap = SPIN_CAP;
    const u64 selm = sel_mask(flags, lane);
    const int cg = i % 20, s0 = i / 20;
    const int ocol = (cg * 2 + (w >> 1)) * 8 + g * 2, otok = (w & 1) * 16 + n;
    {
        u32x4 wd[28];
        const rsrc_t RD = mk(base + OFF_WD + (size_t)cg * 2 * 55296, 2 * 55296);
#pragma unroll
        for (int k = 0; k < 14; ++k)
#pragma unroll
            for (int c = 0; c < 2; ++c) wd[2 * k + c] = ld4p(RD, 14 * w + k < 54 ? lane * 16 : OOB, c * 55296 + (14 * w + k) * 1024);
        int it = 0;
#pragma unroll 1
        for (int s = s0; s < NSEG; s += NOS, ++it) {
            wait_ge(flags + F_H + s, 14u, cap, true);
            const rsrc_t RH = mk(base + OFF_HF + (size_t)s * 110592, 110592);
            u32x4 hb[28];
#pragma unroll
            for (int k = 0; k < 14; ++k) {
                const int vo = 14 * w + k < 54 ? lane * 16 : OOB;
                hb[2 * k] = ld4(RH, vo, (14 * w + k) * 1024); hb[2 * k + 1] = ld4(RH, vo, 55296 + (14 * w + k) * 1024);
            }
            const u32x2 xv = *(const u32x2*)(x + (s * 32 + otok) * (DM / 2) + ocol);
            f32x4 acc[4];
#pragma unroll
            for (int k = 0; k < 14; ++k)
#pragma unroll
                for (int q = 0; q < 4; ++q) { if (k == 0) MF0_AV(acc[q], wd[q >> 1], hb[q & 1]); else MF_AV(acc[q], wd[2 * k + (q >> 1)], hb[2 * k + (q & 1)]); }
            POST4(acc[0], acc[1], acc[2], acc[3]);
#pragma unroll
            for (int q = 0; q < 4; ++q) *(LAS f32x4*)(L + L_RD + (w * 4 + q) * 1024 + lane * 16) = acc[q];
            BAR();
            f32x4 r = {bflo(xv.x), bfhi(xv.x), bflo(xv.y), bfhi(xv.y)};
#pragma unroll
            for (int k = 0; k < 4; ++k) r += *(LAS f32x4*)(L + L_RD + (k * 4 + w) * 1024 + lane * 16);
            *(LAS f32x4*)(L + L_BASE + it * 4096 + w * 1024 + lane * 16) = r;
            BAR();
        }
    }
    int it = 0;
#pragma unroll 1
    for (int s = s0; s < NSEG; s += NOS, ++it) {
        STAMP_DECL;
        STAMPW(0);
        wait_ge(flags + F_U + 32 * (s & 1) + 8 * w + (lane & 7), (u32)s + 1u, cap, true);
        u32x4 uf[16], mf[4];
        {
            const rsrc_t RU = mk(base + OFF_U + (size_t)s * 1310720 + (size_t)cg * 2 * 32768, 2 * 32768);
#pragma unroll
            for (int k = 0; k < 8; ++k)
#pragma unroll
                for (int c = 0; c < 2; ++c) uf[2 * k + c] = ld4(RU, lane * 16, c * 32768 + (8 * w + k) * 1024);
        }
        wait_ge(flags + F_M + s * 128 + w * 32 + (lane & 7), 1u, cap, true);
        STAMPW(1);
        {
            const rsrc_t RM = mk(m_base(base, s, selm) + (size_t)cg * 2 * 16384, 2 * 16384);
#pragma unroll
            for (int k = 0; k < 2; ++k)
#pragma unroll
                for (int c = 0; c < 2; ++c) mf[2 * k + c] = ld4(RM, lane * 16, c * 16384 + (2 * w + k) * 1024);
        }
        wait_ge(flags + F_PX + s * 128 + w * 32 + (lane & 15), 1u, cap, false);
        STAMPW(2);
        f32x4 acc[4];
        {
            const rsrc_t RP = mk(base + OFF_PX + (size_t)s * 65536, 65536);
            const rsrc_t RQ = mk(base + OFF_PM + (size_t)s * 16384, 16384);
            u32x4 pf[16], pm[4];
#pragma unroll
            for (int k = 0; k < 8; ++k) { pf[2 * k] = ld4(RP, lane * 16, (8 * w + k) * 1024); pf[2 * k + 1] = ld4(RP, lane * 16, 32768 + (8 * w + k) * 1024); }
#pragma unroll
            for (int k = 0; k < 2; ++k) { pm[2 * k] = ld4(RQ, lane * 16, (2 * w + k) * 1024); pm[2 * k + 1] = ld4(RQ, lane * 16, 8192 + (2 * w + k) * 1024); }
#pragma unroll
            for (int k = 0; k < 8; ++k)
#pragma unroll
                for (int q = 0; q < 4; ++q) { if (k == 0) MF0_VV(acc[q], uf[q >> 1], pf[q & 1]); else MF_VV(acc[q], uf[2 * k + (q >> 1)], pf[2 * k + (q & 1)]); }
#pragma unroll
            for (int k = 0; k < 2; ++k)
#pragma unroll
                for (int q = 0; q < 4; ++q) MF_VV(acc[q], mf[2 * k + (q >> 1)], pm[2 * k + (q & 1)]);
        }
        POST4(acc[0], acc[1], acc[2], acc[3]);
        STAMPW(3);
#pragma unroll
        for (int q = 0; q < 4; ++q) *(LAS f32x4*)(L + L_RD + (w * 4 + q) * 1024 + lane * 16) = acc[q];
        BAR();
        f32x4 r = *(LAS f32x4*)(L + L_BASE + it * 4096 + w * 1024 + lane * 16);
#pragma unroll
        for (int k = 0; k < 4; ++k) r += *(LAS f32x4*)(L + L_RD + (k * 4 + w) * 1024 + lane * 16);
        *(u32x2*)(out + (s * 32 + otok) * (DM / 2) + ocol) = (u32x2){pk(r[0], r[1]), pk(r[2], r[3])};
        STAMPW(4);
        BAR();
        STAMP_FLUSH(s);
    }
    if (cap == 1u && lane == 0) __hip_atomic_store(flags + F_TMO, 1u, RLX_AGENT);
}

constexpr int LDS_MAIN = 90112;
enum { M_CHAIN = 1, M_D = 2, M_E1 = 4, M_G = 8, M_E2 = 16, M_O = 32, M_ALL = 63 };

__global__ void __launch_bounds__(256, 1) k_main(const u32* __restrict__ x, u32* __restrict__ out, unsigned char* __restrict__ base, int mask) {
    extern __shared__ __attribute__((aligned(16))) unsigned char lds[];
    LAS unsigned char* L = (LAS unsigned char*)lds;
    const int xg = blockIdx.x & 7, j = blockIdx.x >> 3;
    MARK(0);
    int role, arg, oarg = -1;
    if (xg == 1) { role = 0; arg = j; }
    else if (xg == 3) { role = 1; arg = j; }
    else if (xg < 4) {
        if (j < 20) { role = 2; arg = j + 10 * xg; }
        else if (j < 28) { role = 5; arg = j - 20 + 4 * xg; }
        else { role = 7; arg = 0; oarg = 32 + j - 28 + 2 * xg; }
    }
    else {
        const int i = j * 4 + xg - 4;
        if (i < 64) { role = 3; arg = i; oarg = i < 60 ? 40 + i : -1; }
        else if (i < 96) { role = 4; arg = i - 64; }
        else { role = i - 96 < 14 * NTG ? 6 : 7; arg = i - 96; oarg = i - 96; }
    }
    if (role == 0) { if (mask & M_CHAIN) role_A(base, L, arg); }
    else if (role == 1) { if (mask & M_CHAIN) role_B(base, L, arg); }
    else if (role == 2) { if (mask & M_CHAIN) role_C(base, L, arg); }
    else if (role == 3) { if (mask & M_D) role_D(base, L, arg & 31, arg >> 5); }
    else if (role == 4) { if (mask & M_E1) role_E(base, L, arg); }
    else if (role == 5) { if (mask & M_E2) role_P(base, arg); }
    else if (role == 6) { if (mask & M_G) role_G(base, L, arg); }
    if (oarg >= 0 && (mask & M_O)) role_O(base, L, oarg, x, out);
    MARK(3);
}

#define SPLIT_LAUNCH 0

extern "C" void kernel_launch(void* const* d_in, const int* in_sizes, int n_in, void* d_out, int out_size, void* d_ws, size_t ws_size, hipStream_t stream) {
    (void)in_sizes; (void)n_in; (void)out_size; (void)ws_size;
    unsigned char* base = (unsigned char*)(((size_t)d_ws + (2u << 20) - 1) & ~(size_t)((2u << 20) - 1));
    (void)hipFuncSetAttribute((const void*)k_main, hipFuncAttributeMaxDynamicSharedMemorySize, LDS_MAIN);
    k_prep<<<PB_ALL, 256, 0, stream>>>((const u32*)d_in[0], (const u32*)d_in[1], (const unsigned short*)d_in[2], (const unsigned short*)d_in[3],
                                       (const unsigned short*)d_in[4], (const unsigned short*)d_in[5], (const unsigned short*)d_in[8],
                                       (const unsigned short*)d_in[9], (const unsigned short*)d_in[10], base);
    k_main<<<256, 256, LDS_MAIN, stream>>>((const u32*)d_in[0], (u32*)d_out, base, M_ALL);
}
```

```cpp
#include <hip/hip_runtime.h>
#include <hip/hip_bf16.h>

typedef unsigned u32;
typedef unsigned long long u64;
typedef u32 u32x2 __attribute__((ext_vector_type(2)));
typedef u32 u32x4 __attribute__((ext_vector_type(4)));
typedef u32 u32x8 __attribute__((ext_vector_type(8)));
typedef float f32x2 __attribute__((ext_vector_type(2)));
typedef float f32x4 __attribute__((ext_vector_type(4)));
typedef short s16x4 __attribute__((ext_vector_type(4)));
typedef __bf16 bf16x2 __attribute__((ext_vector_type(2)));
#define LAS __attribute__((address_space(3)))
#define GAS __attribute__((address_space(1)))
#define RLX_AGENT __ATOMIC_RELAXED, __HIP_MEMORY_SCOPE_AGENT
typedef __amdgpu_buffer_rsrc_t rsrc_t;

#define STAMPS 0

constexpr int NSEG = 64, DM = 640, NH = 32, FF = 1728;
constexpr u32 SENT = 0xFFFFFFFFu;
constexpr int OOB = 0x7F000000;
constexpr int AUX_SC1 = 16;
constexpr u32 SPIN_CAP = 400000;

constexpr size_t MB2 = 2u << 20;
constexpr size_t OFF_RING = 0;
constexpr size_t OFF_FLAG = 4 * 65536;
constexpr size_t OFF_DBG  = OFF_FLAG + 131072;
constexpr size_t OFF_CAL  = 3 * 655360;
constexpr int    NMBLK    = 22;
constexpr size_t OFF_MB   = MB2;
constexpr size_t OFF_XF   = OFF_MB + NMBLK * MB2;
constexpr size_t OFF_KX   = OFF_XF + (size_t)NSEG * 40960;
constexpr size_t OFF_KM   = OFF_KX + (size_t)NSEG * NH * 16384;
constexpr size_t OFF_PX   = OFF_KM + (size_t)NSEG * NH * 2048;
constexpr size_t OFF_PM   = OFF_PX + (size_t)NSEG * 65536;
constexpr size_t OFF_HF   = OFF_PM + (size_t)NSEG * 16384;
constexpr size_t OFF_PT   = OFF_HF + (size_t)NSEG * 110592;
constexpr size_t OFF_U    = OFF_PT + (size_t)NSEG * 81920;
constexpr size_t WS_END   = OFF_U + (size_t)NSEG * 1310720;
static_assert(WS_END + MB2 <= (256u << 20), "workspace");
static_assert(OFF_DBG + 524288 + 4096 <= OFF_CAL && OFF_CAL + 16384 <= MB2, "block 0");
constexpr int F_H = 320  , F_SEL = 448  , F_TMO = 512;
constexpr int F_KM = 1024;
constexpr int F_M = 2048;
constexpr int F_PX = F_M + 8192;
constexpr int F_PT = F_PX + 8192;
constexpr int F_K = F_PT + 2048;
constexpr int F_U = F_K + 1024;
constexpr int F_WORDS = F_U + 2048;
constexpr int O_BELL = 30720;
#define BELL_NOPS 22
#define BELL2_NOPS 24
#define BELL_M 4

__device__ __forceinline__ int xp(int o) { return ((o >> 13) << 14) | (o & 0x1fff); }
__device__ __forceinline__ rsrc_t mk(const void* p, u32 bytes) { return __builtin_amdgcn_make_buffer_rsrc((void*)p, 0, bytes, 0x00020000); }
__device__ __forceinline__ u32x4 ld4(rsrc_t r, int off, int so = 0) { return __builtin_amdgcn_raw_buffer_load_b128(r, off, so, AUX_SC1); }
__device__ __forceinline__ u32x2 ld2(rsrc_t r, int off, int so = 0) { return __builtin_amdgcn_raw_buffer_load_b64(r, off, so, AUX_SC1); }
__device__ __forceinline__ u32x4 ld4p(rsrc_t r, int off, int so = 0) { return __builtin_amdgcn_raw_buffer_load_b128(r, off, so, 0); }
__device__ __forceinline__ void st4(rsrc_t r, int off, int so, u32x4 v) { __builtin_amdgcn_raw_buffer_store_b128(v, r, off, so, AUX_SC1); }
__device__ __forceinline__ void st2(rsrc_t r, int off, int so, u32x2 v) { __builtin_amdgcn_raw_buffer_store_b64(v, r, off, so, AUX_SC1); }
__device__ __forceinline__ void st1(rsrc_t r, int off, int so, u32 v) { __builtin_amdgcn_raw_buffer_store_b32(v, r, off, so, AUX_SC1); }
__device__ __forceinline__ u32 pk(float a, float b) { f32x2 f = {a, b}; return __builtin_bit_cast(u32, __builtin_convertvector(f, bf16x2)); }
__device__ __forceinline__ u32 pks(float a, float b) { u32 v = pk(a, b); return v < 0xFFFFFFFEu ? v : 0xFFFFFFFEu; }
__device__ __forceinline__ float bflo(u32 v) { return __builtin_bit_cast(float, v << 16); }
__device__ __forceinline__ float bfhi(u32 v) { return __builtin_bit_cast(float, v & 0xffff0000u); }
__device__ __forceinline__ float rbf(float a) { return bflo(pk(a, a) & 0xffffu); }
__device__ __forceinline__ u32x8 cat(u32x4 a, u32x4 b) { return __builtin_shufflevector(a, b, 0, 1, 2, 3, 4, 5, 6, 7); }
__device__ __forceinline__ bool ok4(u32x4 v) { return (v.x != SENT) & (v.y != SENT) & (v.z != SENT) & (v.w != SENT); }
__device__ __forceinline__ bool ok2(u32x2 v) { return (v.x != SENT) & (v.y != SENT); }
__device__ __forceinline__ u32x2 trd(LAS unsigned char* p) { return __builtin_bit_cast(u32x2, __builtin_amdgcn_ds_read_tr16_b64_v4i16((LAS s16x4*)p)); }
__device__ __forceinline__ float fb(u32 v) { return __builtin_bit_cast(float, v); }
__device__ __forceinline__ u32 bf(float v) { return __builtin_bit_cast(u32, v); }
__device__ __forceinline__ int rmap(int m, int hi) { return 4 * (m >> 1) + 2 * hi + (m & 1); }
__device__ __forceinline__ f32x2 fold(f32x4 c) { return (f32x2){c[0], c[1]} + (f32x2){c[2], c[3]}; }
__device__ __forceinline__ u32 pk2(f32x2 f) { return __builtin_bit_cast(u32, __builtin_convertvector(f, bf16x2)); }
__device__ __forceinline__ u32 nos(u32 v) { return v < 0xFFFFFFFEu ? v : 0xFFFFFFFEu; }
__device__ __forceinline__ float hsum(float a, float b) { auto r = __builtin_amdgcn_permlane32_swap(bf(a), bf(b), false, false); return fb(r[0]) + fb(r[1]); }
__device__ __forceinline__ float fmax3(float a, float b, float c) { float r; asm("v_max3_f32 %0, %1, %2, %3" : "=v"(r) : "v"(a), "v"(b), "v"(c)); return r; }
__device__ __forceinline__ float max16(float x) { asm volatile("s_nop 1" : "+v"(x)); auto r = __builtin_amdgcn_permlane16_swap(bf(x), bf(x), false, false); return fmax3(fb(r[0]), fb(r[1]), fb(r[1])); }
__device__ __forceinline__ float max32(float x) { asm volatile("s_nop 1" : "+v"(x)); auto r = __builtin_amdgcn_permlane32_swap(bf(x), bf(x), false, false); return fmax3(fb(r[0]), fb(r[1]), fb(r[1])); }
__device__ __forceinline__ float add16(float x) { auto r = __builtin_amdgcn_permlane16_swap(bf(x), bf(x), false, false); return fb(r[0]) + fb(r[1]); }
__device__ __forceinline__ float add32(float x) { auto r = __builtin_amdgcn_permlane32_swap(bf(x), bf(x), false, false); return fb(r[0]) + fb(r[1]); }
__device__ __forceinline__ float ror8(float x) { return fb(__builtin_amdgcn_update_dpp(0, (int)bf(x), 0x128, 0xf, 0xf, false)); }
__device__ __forceinline__ float mov_hi8(float old, float src) { return fb(__builtin_amdgcn_update_dpp((int)bf(old), (int)bf(src), 0x128, 0xf, 0xc, false)); }

#define BAR() do { asm volatile("s_waitcnt lgkmcnt(0)" ::: "memory"); __builtin_amdgcn_s_barrier(); asm volatile("" ::: "memory"); } while (0)
#define VMCNT(n) asm volatile("s_waitcnt vmcnt(" #n ")" ::: "memory")
#define SMF_A(c, a, w, idx) asm("v_smfmac_f32_16x16x64_bf16 %0, %1, %2, %3" : "+v"(c) : "v"(a), "a"(w), "v"(idx))
#define SMF_V(c, a, w, idx) asm("v_smfmac_f32_16x16x64_bf16 %0, %1, %2, %3" : "+v"(c) : "v"(a), "v"(w), "v"(idx))
#define MF_AV(c, a, b) asm("v_mfma_f32_16x16x32_bf16 %0, %1, %2, %0" : "+v"(c) : "a"(a), "v"(b))
#define MF_VA(c, a, b) asm("v_mfma_f32_16x16x32_bf16 %0, %1, %2, %0" : "+v"(c) : "v"(a), "a"(b))
#define MF_VV(c, a, b) asm("v_mfma_f32_16x16x32_bf16 %0, %1, %2, %0" : "+v"(c) : "v"(a), "v"(b))
#define MF0_AV(c, a, b) asm("v_mfma_f32_16x16x32_bf16 %0, %1, %2, 0" : "=&v"(c) : "a"(a), "v"(b))
#define MF0_VA(c, a, b) asm("v_mfma_f32_16x16x32_bf16 %0, %1, %2, 0" : "=&v"(c) : "v"(a), "a"(b))
#define MF0_VV(c, a, b) asm("v_mfma_f32_16x16x32_bf16 %0, %1, %2, 0" : "=&v"(c) : "v"(a), "v"(b))
#define SB() __builtin_amdgcn_sched_barrier(0)
#define PRE2(a, b) asm volatile("s_nop 1" : "+v"(a), "+v"(b))
#define PRE3(a, b, c) asm volatile("s_nop 1" : "+v"(a), "+v"(b), "+v"(c))
#define PRE4(a, b, c, d) asm volatile("s_nop 1" : "+v"(a), "+v"(b), "+v"(c), "+v"(d))
#define POST2(a, b) asm volatile("s_nop 7" : "+v"(a), "+v"(b))
#define POST3(a, b, c) asm volatile("s_nop 7" : "+v"(a), "+v"(b), "+v"(c))
#define POST4(a, b, c, d) asm volatile("s_nop 7" : "+v"(a), "+v"(b), "+v"(c), "+v"(d))
#define Z4 (f32x4){0.f, 0.f, 0.f, 0.f}

#define STAMP_DECL do {} while (0)
#define STAMP(k) do {} while (0)
#define STAMPW(k) do {} while (0)
#define MARK(k) do {} while (0)
#define STAMP_SET(k, v) do {} while (0)
#define STAMP_FLUSH(s) do {} while (0)
#define STAMP_FLUSH2(s) do {} while (0)

__device__ __forceinline__ u32 wait_ge(GAS u32* word, u32 want, u32& cap, bool sleep) {
    u32 v, spins = cap;
    for (;;) {
        v = __hip_atomic_load(word, RLX_AGENT);
        if (__builtin_amdgcn_ballot_w64(v < want) == 0 || --spins == 0) break;
        if (sleep) __builtin_amdgcn_s_sleep(4);
    }
    cap = spins ? cap : 1u;
    __builtin_amdgcn_fence(__ATOMIC_ACQUIRE, "wavefront");
    return v;
}
__device__ __forceinline__ u32 mx3(u32 a, u32 b, u32 c) { return max(max(a, b), c); }
__device__ __forceinline__ u32 mx4(u32 t, u32x4 v) { return mx3(mx3(t, v.x, v.y), v.z, v.w); }
__device__ __forceinline__ u64 sel_mask(GAS u32* flags, int lane) {
    const int d = (int)flags[F_SEL + (lane & 31)], ad = d < 0 ? -d : d;
    const bool clear = lane <= NMBLK && ad >= 110 && ad <= 400;
    const u64 cm = __builtin_amdgcn_ballot_w64(clear), up = __builtin_amdgcn_ballot_w64(d > 0);
    const u64 odd = 0xAAAAAAAAAAAAAAAAull;
    const bool maj = 2 * __builtin_popcountll((up ^ odd) & cm) > __builtin_popcountll(cm);
    return (up & cm) | ((maj ? ~odd : odd) & ~cm);
}
__device__ __forceinline__ unsigned char* m_base(unsigned char* base, int s, u64 selm) {
    const int b = s / 3;
    return base + OFF_MB + (size_t)b * MB2 + (s - 3 * b) * 655360 + (((selm >> (b + 1)) & 1) << 13);
}
__device__ __forceinline__ u32 wave_min(u32 v) {
    v = min(v, (u32)__builtin_amdgcn_update_dpp(-1, (int)v, 0x111, 0xf, 0xf, false));
    v = min(v, (u32)__builtin_amdgcn_update_dpp(-1, (int)v, 0x112, 0xf, 0xf, false));
    v = min(v, (u32)__builtin_amdgcn_update_dpp(-1, (int)v, 0x114, 0xf, 0xf, false));
    return (u32)__builtin_amdgcn_readlane((int)v, 7);
}

constexpr int PB_CAL = 48, PB_LN = 256, PB_FILL = 102;
constexpr int PB_ALL = PB_CAL + PB_LN + PB_FILL;

__global__ void __launch_bounds__(256) k_prep(const u32* __restrict__ x, const u32* __restrict__ mem0, unsigned char* __restrict__ base) {
    __shared__ u32 best[4];
    const int tid = threadIdx.x, lane = tid & 63, wave = __builtin_amdgcn_readfirstlane(tid >> 6);
    int b = blockIdx.x;
    if (b < PB_CAL) {
        const int blk = (b >> 3) * 4 + (b & 3);
        if ((b & 4) || blk > NMBLK) return;
        GAS u32* cal = (GAS u32*)(base + (size_t)blk * MB2 + OFF_CAL) + (wave & 1) * 2048 + (wave >> 1) * 512;
        if (lane < 8) __hip_atomic_store(cal + lane * 64, 0u, RLX_AGENT);
        VMCNT(0);
        u32 m = 100000u;
#pragma unroll
        for (int i = 0; i < 5; ++i) {
            u64 t0, t1; u32 v;
            asm volatile("s_memtime %0\n s_waitcnt lgkmcnt(0)\n global_load_dword %2, %3, off sc1\n s_waitcnt vmcnt(0)\n s_memtime %1\n s_waitcnt lgkmcnt(0)"
                         : "=&s"(t0), "=&s"(t1), "=&v"(v) : "v"(cal + i * 64) : "memory");
            const u32 dt = (u32)(t1 - t0);
            if (dt > 310u) m = min(m, dt);
        }
        if (lane == 0) best[wave] = m;
        __syncthreads();
        if (tid == 0) ((int*)(base + OFF_FLAG))[F_SEL + blk] = (int)min(best[0], best[2]) - (int)min(best[1], best[3]);
        return;
    }
    b -= PB_CAL;
    if (b < PB_LN) {
        const int tok = b * 8 + (tid >> 5), c = tid & 31;
        const u32x4* row = (const u32x4*)(x + (size_t)tok * (DM / 2));
        u32x4 v[3];
        v[0] = row[c]; v[1] = row[c + 32]; v[2] = c < 16 ? row[c + 64] : (u32x4){0u, 0u, 0u, 0u};
        float s = 0.f;
#pragma unroll
        for (int i = 0; i < 3; ++i)
#pragma unroll
            for (int j = 0; j < 4; ++j) s += bflo(v[i][j]) + bfhi(v[i][j]);
        s += __shfl_xor(s, 1); s += __shfl_xor(s, 2); s += __shfl_xor(s, 4); s += __shfl_xor(s, 8); s += __shfl_xor(s, 16);
        const float mean = s * (1.f / DM);
        float q = 0.f;
#pragma unroll
        for (int i = 0; i < 3; ++i)
#pragma unroll
            for (int j = 0; j < 4; ++j) {
                const float d0 = bflo(v[i][j]) - mean, d1 = bfhi(v[i][j]) - mean;
                if (i < 2 || c < 16) q += d0 * d0 + d1 * d1;
            }
        q += __shfl_xor(q, 1); q += __shfl_xor(q, 2); q += __shfl_xor(q, 4); q += __shfl_xor(q, 8); q += __shfl_xor(q, 16);
        const float mb = rbf(mean), den = rbf(sqrtf(rbf(rbf(q * (1.f / DM)) + 1e-05f)));
        unsigned char* dst = base + OFF_XF + (size_t)(tok >> 5) * 40960 + ((tok >> 4) & 1) * 1024 + (tok & 15) * 16;
#pragma unroll
        for (int i = 0; i < 3; ++i) {
            if (i == 2 && c >= 16) break;
            u32x4 o;
#pragma unroll
            for (int j = 0; j < 4; ++j) o[j] = pk(rbf(bflo(v[i][j]) - mb) / den, rbf(bfhi(v[i][j]) - mb) / den);
            const int ch = c + 32 * i;
            *(u32x4*)(dst + (ch >> 2) * 2048 + (ch & 3) * 256) = o;
        }
        return;
    }
    b -= PB_LN;
    const u32x4 sv = {SENT, SENT, SENT, SENT};
    constexpr int N0 = 16384, N1 = N0 + 81920, N2 = N1 + F_WORDS / 4;
    static_assert(N2 <= PB_FILL * 1024 && F_SEL % 4 == 0, "fill");
#pragma unroll
    for (int k = 0; k < 4; ++k) {
        const int i = (b * 4 + k) * 256 + tid;
        if (i < N0) {
            const int o = i * 16, lo = ((o >> 14) << 13) | (o & 0x1fff);
            u32x4 v = sv;
            if (lo >= 20480 && lo < 30720) {
                const int cs = (lo - 20480) >> 8, r = (lo >> 4) & 15;
#pragma unroll
                for (int q = 0; q < 4; ++q) v[q] = nos(mem0[((2 * (r >> 2) + (r & 1)) * DM + cs * 16 + 4 * q + (r & 2)) >> 1]);
            }
            *(u32x4*)(base + OFF_RING + o) = v;
        } else if (i < N1) {
            *(u32x4*)(base + OFF_MB + (size_t)(i - N0) * 16) = sv;
        } else if (i < N2) {
            const int f = (i - N1) * 4;
            if (f < F_SEL || f >= F_SEL + 32) *(u32x4*)(base + OFF_FLAG + f * 4) = (u32x4){0u, 0u, 0u, 0u};
        }
    }
}

struct Wts { const unsigned char *q, *k, *v, *o, *g, *u, *d; };
constexpr int L_WR = 40960;
__device__ __forceinline__ void dmag(const unsigned char* sbase, u32 vo, u32 dst) {
    asm volatile("s_mov_b32 m0, %2\n\ts_nop 0\n\tglobal_load_lds_dwordx4 %0, %1" :: "v"(vo), "s"(sbase), "s"(dst) : "memory");
}
__device__ __forceinline__ void vm_wait(int n) {
    switch (n) {
        case 0: VMCNT(0); break; case 4: VMCNT(4); break; case 12: VMCNT(12); break; case 8: VMCNT(8); break; case 10: VMCNT(10); break; case 16: VMCNT(16); break;
        case 18: VMCNT(18); break; default: VMCNT(20); break;
    }
}
__device__ __forceinline__ u32x4 frag(LAS unsigned char* p, int second) { const u32x2 a = trd(p), b = trd(p + second); return (u32x4){a.x, a.y, b.x, b.y}; }
template <int NU, class CNT, class ISSUE, class TAKE>
__device__ __forceinline__ void ring_load(LAS unsigned char* L, int w, CNT cnt, ISSUE issue, TAKE take) {
    const u32 r0 = L_WR + w * 30720;
#pragma unroll
    for (int u = 0; u < 3 && u < NU; ++u) issue(u, r0 + u * 10240);
#pragma unroll
    for (int u = 0; u < NU; ++u) {
        vm_wait((u + 1 < NU ? cnt(u + 1) : 0) + (u + 2 < NU ? cnt(u + 2) : 0));
        take(u, L + r0 + (u % 3) * 10240);
        if (u + 3 < NU) {
            asm volatile("s_waitcnt lgkmcnt(0)" ::: "memory");
            issue(u + 3, r0 + (u % 3) * 10240);
        }
    }
    asm volatile("s_waitcnt lgkmcnt(0)" ::: "memory");
}
#define ISSUE8(pa, pb, ld, vo, dst)                                                                                      \
    do {                                                                                                                 \
        _Pragma("unroll") for (int i_ = 0; i_ < 4; ++i_) dmag((pa) + (size_t)i_ * 16 * (ld), vo, (dst) + i_ * 1024);     \
        _Pragma("unroll") for (int i_ = 0; i_ < 4; ++i_) dmag((pb) + (size_t)i_ * 16 * (ld), vo, (dst) + 4096 + i_ * 1024); \
    } while (0)
#define ISSUE8W(pa, ld, vo, dst) do { _Pragma("unroll") for (int i_ = 0; i_ < 8; ++i_) dmag((pa) + (size_t)i_ * 8 * (ld), vo, (dst) + i_ * 1024); } while (0)
#define WO_SETUP()                                                                                                       \
    u32 wvo[5];                                                                                                          \
    _Pragma("unroll") for (int i_ = 0; i_ < 5; ++i_) { const u32 b_ = i_ * 1024 + lane * 16; wvo[i_] = (b_ / 320) * 1280 + b_ % 320; } \
    const unsigned char* const wo_ = W.o + (size_t)h * 128 * 1280 + w * 320
#define ISSUE_WO(es, dst)                                                                                                \
    do { _Pragma("unroll") for (int i_ = 0; i_ < 10; ++i_) dmag(wo_ + ((es) * 32 + (i_ / 5) * 16) * 1280, wvo[i_ % 5], (dst) + i_ * 1024); } while (0)

constexpr int L_MEM = 0, L_IMG = 10240;

__device__ __forceinline__ int kstep(int w, int p) { return p < 2 ? w + 4 * p : (p < 4 ? 6 + p : ((w + (p >> 1) - 1) & 3) + 4 * (p & 1)); }
#define GATHER_MEM()                                                                                                     \
    do {                                                                                                                 \
        u32x4 g2;                                                                                                        \
        u32 spins = cap;                                                                                                 \
        if (BELL2_NOPS >= 0 && s > 0) {                                                                                  \
            for (;;) {                                                                                                   \
                asm volatile("" ::: "memory");                                                                           \
                const u32 b_ = __builtin_amdgcn_raw_buffer_load_b32(RR, obell2, sel, AUX_SC1);                           \
                if (__builtin_amdgcn_ballot_w64((int)b_ < s) == 0) break;                                                \
                asm volatile("" : "+s"(spins));                                                                          \
                if (--spins == 0) break;                                                                                 \
            }                                                                                                            \
            STAMP(5);                                                                                                    \
            cap = spins ? cap : 1u;                                                                                      \
            spins = cap;                                                                                                 \
            _Pragma("unroll") for (int i_ = 0; i_ < BELL2_NOPS; ++i_) asm volatile("s_nop 7");                           \
            STAMP(6);                                                                                                    \
        }                                                                                                                \
        for (;;) {                                                                                                       \
            asm volatile("" ::: "memory");                                                                               \
            g2 = ld4(RR, mo2, slot); a[0] = ld4(RR, mo0, slot); a[1] = ld4(RR, mo1, slot);                               \
            const u32 t_ = mx4(mx4(mx4(0u, g2), a[0]), a[1]);                                                            \
            if (__builtin_amdgcn_ballot_w64(t_ == SENT) == 0) break;                                                     \
            asm volatile("" : "+s"(spins));                                                                              \
            if (--spins == 0) break;                                                                                     \
        }                                                                                                                \
        STAMP(0);                                                                                                        \
        STAMP_SET(7, cap - spins);                                                                                       \
        *(LAS u32x4*)(lw) = a[0];                                                                                        \
        *(LAS u32x4*)(lw + 4096) = a[1];                                                                                 \
        if (w < 2) *(LAS u32x4*)(lw + 8192) = g2;                                                                        \
        cap = spins ? cap : 1u;                                                                                          \
        SB();                                                                                                            \
    } while (0)
#define READ_MEM()                                                                                                       \
    do {                                                                                                                 \
        SB();                                                                                                            \
        BAR();                                                                                                           \
        STAMP(1);                                                                                                        \
        a[2] = *(LAS u32x4*)(l0 + 8192); SB(); a[3] = *(LAS u32x4*)(l0 + 9216); SB();                                    \
        a[4] = *(LAS u32x4*)(l1); SB(); a[5] = *(LAS u32x4*)(l1 + 4096); SB();                                           \
        a[6] = *(LAS u32x4*)(l2); SB(); a[7] = *(LAS u32x4*)(l2 + 4096); SB();                                           \
        a[8] = *(LAS u32x4*)(l3); SB(); a[9] = *(LAS u32x4*)(l3 + 4096); SB();                                           \
    } while (0)
#define CHAIN_SETUP()                                                                                                    \
    const int tid = threadIdx.x, lane = tid & 63, w = __builtin_amdgcn_readfirstlane(tid >> 6);                          \
    const int n = lane & 15, g = lane >> 4;                                                                              \
    GAS u32* flags = (GAS u32*)(base + OFF_FLAG);                                                                        \
    const u64 selm = sel_mask(flags, lane);                                                                              \
    const int sel = (int)(selm & 1) << 13;                                                                               \
    const rsrc_t RR = mk(base + OFF_RING, 4 * 65536);                                                                    \
    const u32 idx = (lane & 2) ? 0xEEEEu : 0x4444u;                                                                      \
    const u32x4 sent4 = {SENT, SENT, SENT, SENT};                                                                        \
    u32 cap = SPIN_CAP
#define MEM_SETUP()                                                                                                      \
    const int mo0 = xp(20480 + w * 1024 + lane * 16), mo1 = xp(20480 + (w + 4) * 1024 + lane * 16),                      \
              mo2 = w < 2 ? xp(20480 + (w + 8) * 1024 + lane * 16) : OOB;                                                \
    const int obell2 = xp(O_BELL + 512 + w * 128);                                                                       \
    LAS unsigned char* const l0 = L + L_MEM + lane * 16;                                                                 \
    LAS unsigned char* const lw = l0 + w * 1024;                                                                         \
    LAS unsigned char* const l1 = l0 + ((w + 1) & 3) * 1024;                                                             \
    LAS unsigned char* const l2 = l0 + ((w + 2) & 3) * 1024;                                                             \
    LAS unsigned char* const l3 = l0 + ((w + 3) & 3) * 1024
#define CLK0() do {} while (0)
#define CLK1() do {} while (0)

__device__ __forceinline__ void role_A(unsigned char* base, LAS unsigned char* L, const Wts& W, const int h) {
    CHAIN_SETUP();
    MEM_SETUP();
    u32x8 wt[40];
    {
        const u32 vo = (lane >> 3) * 8192 + (lane & 7) * 16, to = (8 * g + (n >> 2)) * 128 + (n & 3) * 8;
        const unsigned char* src = (w < 2 ? W.q : W.k) + h * 256 + (w & 1) * 128;
        ring_load<10>(L, w, [](int) { return 8; },
            [&](int u, u32 dst) { ISSUE8W(src + (size_t)kstep(w, u) * 64 * 8192, 8192, vo, dst); },
            [&](int u, LAS unsigned char* p) {
#pragma unroll
                for (int t = 0; t < 4; ++t) { LAS unsigned char* q = p + to + t * 32; wt[u * 4 + t] = cat(frag(q, 512), frag(q + 4096, 512)); } });
    }
    BAR();
    MARK(1);
    u32 knownK = 0;
    u32x4 kx[8];
    if (w == 0) {
        knownK = (u32)__builtin_amdgcn_readfirstlane((int)wait_ge(flags + F_K + h * 32, 1u, cap, false));
        const rsrc_t RK = mk(base + OFF_KX + (size_t)h * 16384, 8192);
#pragma unroll
        for (int i = 0; i < 8; ++i) kx[i] = ld4(RK, lane * 16 + i * 1024);
    }
    const float ninf = g < 2 ? -INFINITY : 0.f;
    const f32x4 t0init = {ninf, ninf, ninf, ninf};
    const u32 ipa = L_IMG + (4 * g + (n >> 2)) * 32 + (n & 3) * 8;
    const int pox = xp(h * 512 + (g >> 1) * 256 + rmap(n & 7, g & 1) * 16 + (n >> 3) * 8);
    const int pom = g >= 2 ? xp(16384 + h * 128 + rmap(n & 7, n >> 3) * 8 + (g - 2) * 4) : OOB;
    LAS unsigned char* const iw = L + L_IMG + (64 * (w & 1) + n) * 32 + 16 * (w >> 1) + 4 * g;
    const int pclr = lane < 40 ? xp(lane < 32 ? h * 512 + lane * 16 : 16384 + h * 128 + (lane - 32) * 16) : OOB;
    const int obell = (tid == 192 && (h & 7) == 0) ? xp(O_BELL + (h >> 3) * 128) : OOB;
    const float cfs = 0.08838834764831845f * 1.4426950408889634f;
    const f32x2 cf2 = {cfs, cfs};
    CLK0();
#pragma unroll 1
    for (int s = 0; s < NSEG; ++s) {
        const int slot = (s & 3) * 65536 + sel;
        STAMP_DECL;
        u32x4 a[10];
        f32x4 c0 = Z4, c1 = Z4, c2 = Z4, c3 = Z4;
        PRE4(c0, c1, c2, c3);
        GATHER_MEM();
#pragma unroll
        for (int j = 0; j < 10; ++j) {
            if (j == 2) READ_MEM();
            if (j == 3) { SB(); st1(RR, obell, sel, (u32)s + 1u); SB(); }
            if (j < 8) { SMF_A(c0, a[j], wt[j * 4], idx); SMF_A(c2, a[j], wt[j * 4 + 2], idx); SMF_A(c1, a[j], wt[j * 4 + 1], idx); SMF_A(c3, a[j], wt[j * 4 + 3], idx); }
            else { SMF_V(c0, a[j], wt[j * 4], idx); SMF_V(c2, a[j], wt[j * 4 + 2], idx); SMF_V(c1, a[j], wt[j * 4 + 1], idx); SMF_V(c3, a[j], wt[j * 4 + 3], idx); }
        }
        POST4(c0, c1, c2, c3);
        STAMP(2);
        *(LAS u32*)(iw) = pk2(fold(c0));
        *(LAS u32*)(iw + 1024) = pk2(fold(c2));
        *(LAS u32*)(iw + 512) = pk2(fold(c1));
        *(LAS u32*)(iw + 1536) = pk2(fold(c3));
        BAR();
        STAMP(3);
        if (w == 0) {
            if (s < NSEG - 1) {
                asm volatile(
                    "ds_read_b64_tr_b16 v[216:217], %[ip]\n\t"
                    "ds_read_b64_tr_b16 v[218:219], %[ip] offset:512\n\t"
                    "ds_read_b64_tr_b16 v[220:221], %[ip] offset:1024\n\t"
                    "ds_read_b64_tr_b16 v[222:223], %[ip] offset:1536\n\t"
                    "ds_read_b64_tr_b16 v[224:225], %[ip] offset:2048\n\t"
                    "ds_read_b64_tr_b16 v[226:227], %[ip] offset:2560\n\t"
                    "ds_read_b64_tr_b16 v[228:229], %[ip] offset:3072\n\t"
                    "ds_read_b64_tr_b16 v[230:231], %[ip] offset:3584\n\t"
                    "s_waitcnt lgkmcnt(6)\n\t"
                    "v_mfma_f32_16x16x32_bf16 v[240:243], v[216:219], v[216:219], %[ti]\n\t"
                    "v_mfma_f32_16x16x32_bf16 v[232:235], %[k0], v[216:219], 0\n\t"
                    "v_mfma_f32_16x16x32_bf16 v[236:239], %[k4], v[216:219], 0\n\t"
                    "s_waitcnt lgkmcnt(4)\n\t"
                    "v_mfma_f32_16x16x32_bf16 v[240:243], v[220:223], v[220:223], v[240:243]\n\t"
                    "v_mfma_f32_16x16x32_bf16 v[232:235], %[k1], v[220:223], v[232:235]\n\t"
                    "v_mfma_f32_16x16x32_bf16 v[236:239], %[k5], v[220:223], v[236:239]\n\t"
                    "s_waitcnt lgkmcnt(2)\n\t"
                    "v_mfma_f32_16x16x32_bf16 v[240:243], v[224:227], v[224:227], v[240:243]\n\t"
                    "v_mfma_f32_16x16x32_bf16 v[232:235], %[k2], v[224:227], v[232:235]\n\t"
                    "v_mfma_f32_16x16x32_bf16 v[236:239], %[k6], v[224:227], v[236:239]\n\t"
                    "s_waitcnt lgkmcnt(0)\n\t"
                    "v_mfma_f32_16x16x32_bf16 v[240:243], v[228:231], v[228:231], v[240:243]\n\t"
                    "v_mfma_f32_16x16x32_bf16 v[232:235], %[k3], v[228:231], v[232:235]\n\t"
                    "v_mfma_f32_16x16x32_bf16 v[236:239], %[k7], v[228:231], v[236:239]\n\t"
                    "s_nop 5\n\t"
                    "v_mov_b32_dpp v240, v242 row_ror:8 row_mask:0xf bank_mask:0xc\n\t"
                    "v_mov_b32_dpp v241, v243 row_ror:8 row_mask:0xf bank_mask:0xc\n\t"
                    "v_mov_b32_dpp v232, v236 row_ror:8 row_mask:0xf bank_mask:0xc\n\t"
                    "v_mov_b32_dpp v233, v237 row_ror:8 row_mask:0xf bank_mask:0xc\n\t"
                    "v_mov_b32_dpp v234, v238 row_ror:8 row_mask:0xf bank_mask:0xc\n\t"
                    "v_mov_b32_dpp v235, v239 row_ror:8 row_mask:0xf bank_mask:0xc\n\t"
                    "v_max3_f32 v244, v240, v241, v232\n\t"
                    "v_max3_f32 v244, v244, v233, v234\n\t"
                    "v_max_f32 v244, v244, v235\n\t"
                    "s_nop 1\n\t"
                    "v_max_f32_dpp v244, v244, v244 row_ror:8 row_mask:0xf bank_mask:0xf\n\t"
                    "v_mov_b32 v245, v244\n\t"
                    "s_nop 1\n\t"
                    "v_permlane16_swap_b32 v244, v245\n\t"
                    "v_max_f32 v244, v244, v245\n\t"
                    "v_mov_b32 v245, v244\n\t"
                    "s_nop 1\n\t"
                    "v_permlane32_swap_b32 v244, v245\n\t"
                    "v_max_f32 v244, v244, v245\n\t"
                    "v_pk_add_f32 v[232:233], v[232:233], v[244:245] op_sel_hi:[1,0] neg_lo:[0,1] neg_hi:[0,1]\n\t"
                    "v_pk_add_f32 v[234:235], v[234:235], v[244:245] op_sel_hi:[1,0] neg_lo:[0,1] neg_hi:[0,1]\n\t"
                    "v_pk_add_f32 v[240:241], v[240:241], v[244:245] op_sel_hi:[1,0] neg_lo:[0,1] neg_hi:[0,1]\n\t"
                    "v_pk_mul_f32 v[232:233], v[232:233], %[cf]\n\t"
                    "v_pk_mul_f32 v[234:235], v[234:235], %[cf]\n\t"
                    "v_pk_mul_f32 v[240:241], v[240:241], %[cf]\n\t"
                    "v_exp_f32 v232, v232\n\t"
                    "v_exp_f32 v233, v233\n\t"
                    "v_exp_f32 v234, v234\n\t"
                    "v_exp_f32 v235, v235\n\t"
                    "v_exp_f32 v240, v240\n\t"
                    "v_exp_f32 v241, v241\n\t"
                    "v_pk_add_f32 v[244:245], v[232:233], v[234:235]\n\t"
                    "v_pk_add_f32 v[244:245], v[244:245], v[240:241]\n\t"
                    "v_add_f32 v244, v244, v245\n\t"
                    "s_nop 1\n\t"
                    "v_add_f32_dpp v244, v244, v244 row_ror:8 row_mask:0xf bank_mask:0xf\n\t"
                    "v_mov_b32 v245, v244\n\t"
                    "s_nop 1\n\t"
                    "v_permlane16_swap_b32 v244, v245\n\t"
                    "v_add_f32 v244, v244, v245\n\t"
                    "v_mov_b32 v245, v244\n\t"
                    "s_nop 1\n\t"
                    "v_permlane32_swap_b32 v244, v245\n\t"
                    "v_add_f32 v244, v244, v245\n\t"
                    "v_rcp_f32 v244, v244\n\t"
                    "s_nop 0\n\t"
                    "v_pk_mul_f32 v[232:233], v[232:233], v[244:245] op_sel_hi:[1,0]\n\t"
                    "v_pk_mul_f32 v[234:235], v[234:235], v[244:245] op_sel_hi:[1,0]\n\t"
                    "v_pk_mul_f32 v[240:241], v[240:241], v[244:245] op_sel_hi:[1,0]\n\t"
                    "v_cvt_pk_bf16_f32 v232, v232, v233\n\t"
                    "v_cvt_pk_bf16_f32 v233, v234, v235\n\t"
                    "buffer_store_dwordx2 v[232:233], %[px], %[rr], %[so] offen sc1\n\t"
                    "v_cvt_pk_bf16_f32 v240, v240, v241\n\t"
                    "buffer_store_dword v240, %[pm], %[rr], %[so] offen sc1\n\t"
                    :
                    : [ip] "v"(ipa), [ti] "v"(t0init), [k0] "v"(kx[0]), [k1] "v"(kx[1]), [k2] "v"(kx[2]), [k3] "v"(kx[3]), [k4] "v"(kx[4]),
                      [k5] "v"(kx[5]), [k6] "v"(kx[6]), [k7] "v"(kx[7]), [cf] "v"(cf2), [px] "v"(pox), [pm] "v"(pom), [rr] "s"(RR), [so] "s"(slot)
                    : "memory", "v216", "v217", "v218", "v219", "v220", "v221", "v222", "v223", "v224", "v225", "v226", "v227", "v228", "v229", "v230",
                      "v231", "v232", "v233", "v234", "v235", "v236", "v237", "v238", "v239", "v240", "v241", "v242", "v243", "v244", "v245");
                STAMPW(4);
                st4(RR, pclr, ((s + 2) & 3) * 65536 + sel, sent4);
                if (knownK <= (u32)s + 1u) knownK = (u32)__builtin_amdgcn_readfirstlane((int)wait_ge(flags + F_K + h * 32, (u32)s + 2u, cap, false));
                const rsrc_t RK = mk(base + OFF_KX + ((size_t)(s + 1) * NH + h) * 16384, 8192);
#pragma unroll
                for (int i = 0; i < 8; ++i) kx[i] = ld4(RK, lane * 16 + i * 1024);
            }
        } else if (w == 1) {
            const rsrc_t RKM = mk(base + OFF_KM + ((size_t)s * NH + h) * 2048, 2048);
#pragma unroll
            for (int e = 0; e < 4; ++e) {
                const u32x2 lo = trd(L + ipa + e * 1024), hi = trd(L + ipa + e * 1024 + 512);
                st4(RKM, n >= 8 ? e * 512 + (g * 8 + n - 8) * 16 : OOB, 0, (u32x4){lo.x, lo.y, hi.x, hi.y});
            }
            VMCNT(0);
            if (lane == 0) __hip_atomic_store(flags + F_KM + h * 32, (u32)s + 1u, RLX_AGENT);
        }
        STAMP_FLUSH(s);
    }
    CLK1();
    if (cap == 1u && lane == 0) __hip_atomic_store(flags + F_TMO, 1u, RLX_AGENT);
}

__device__ __forceinline__ void role_B(unsigned char* base, LAS unsigned char* L, const Wts& W, const int h) {
    CHAIN_SETUP();
    MEM_SETUP();
    u32x8 wt[40];
    {
        const u32 vo = (lane >> 2) * 8192 + (lane & 3) * 16, to = (8 * g + (n >> 2)) * 64 + (n & 3) * 8, tp = (4 * g + (n >> 2)) * 320 + (n & 3) * 8;
        const size_t co = h * 256 + w * 64;
        WO_SETUP();
        u32x4 lo[10];
        ring_load<9>(L, w, [](int u) { return u < 5 ? 8 : 10; },
            [&](int u, u32 dst) {
                if (u < 5) ISSUE8(W.v + co + (size_t)kstep(w, 2 * u) * 64 * 8192, W.v + co + (size_t)kstep(w, 2 * u + 1) * 64 * 8192, 8192, vo, dst);
                else ISSUE_WO(u - 5, dst); },
            [&](int u, LAS unsigned char* p) {
                if (u < 5) {
#pragma unroll
                    for (int t = 0; t < 4; ++t) { LAS unsigned char* q = p + to + (t >> 1) * 4096 + (t & 1) * 32; wt[u * 4 + t] = cat(frag(q, 256), frag(q + 2048, 256)); }
                } else {
#pragma unroll
                    for (int i = 0; i < 10; ++i) {
                        const u32x4 f = frag(p + tp + i * 32, 16 * 320);
                        if (((u - 5) & 1) == 0) lo[i] = f; else wt[20 + 2 * i + ((u - 5) >> 1)] = cat(lo[i], f);
                    }
                } });
    }
    BAR();
    MARK(1);
    LAS unsigned char* const ip = L + L_IMG + (16 * (g >> 1) + 4 * (g & 1) + ((n >> 2) & 1) + 8 * (n >> 3)) * 32 + 8 * (n & 3);
    LAS unsigned char* const iw = L + L_IMG + (16 * w + 2 * (n >> 2) + (n & 1)) * 32 + 8 * g + 2 * (n & 2);
    const int mso = (10 * w + (g & 1)) * 16384 + h * 256 + n * 16 + (g >> 1) * 8;
    const int mcl = (10 * w + g) * 16384 + h * 256 + n * 16, mcl2 = g < 2 ? mcl : OOB;
    CLK0();
#pragma unroll 1
    for (int s = 0; s < NSEG; ++s) {
        const int slot = (s & 3) * 65536 + sel;
        STAMP_DECL;
        const rsrc_t RM = mk(m_base(base, s, selm), 655360);
        u32x4 a[10];
        f32x4 c0 = Z4, c1 = Z4, c[10];
#pragma unroll
        for (int i = 0; i < 10; ++i) c[i] = Z4;
        PRE4(c[0], c[1], c[2], c[3]); PRE4(c[4], c[5], c[6], c[7]); PRE4(c[8], c[9], c0, c1);
        GATHER_MEM();
#pragma unroll
        for (int j = 0; j < 10; ++j) {
            if (j == 2) READ_MEM();
            SMF_A(c0, a[j], wt[j * 2], idx); SMF_A(c1, a[j], wt[j * 2 + 1], idx);
        }
        POST2(c0, c1);
        STAMP(2);
        *(LAS u32*)(iw) = pk2(fold(c0));
        *(LAS u32*)(iw + 256) = pk2(fold(c1));
        BAR();
        STAMP(3);
        u32x4 av[2];
#pragma unroll
        for (int t = 0; t < 2; ++t) { const u32x2 lo = trd(ip + t * 1024), hi = trd(ip + t * 1024 + 64); av[t] = (u32x4){lo.x, lo.y, hi.x, hi.y}; }
#define B_MF(i, t) do { if ((i) < 6) SMF_A(c[i], av[t], wt[20 + 2 * (i) + (t)], idx); else SMF_V(c[i], av[t], wt[20 + 2 * (i) + (t)], idx); SB(); } while (0)
#define B_E1(i) do { asm volatile("v_pk_add_f32 %0, %2, %3\n\tv_pk_add_f32 %1, %4, %5" : "=&v"(f0), "=&v"(f1) : "v"(__builtin_shufflevector(c[i], c[i], 0, 1)), \
            "v"(__builtin_shufflevector(c[i], c[i], 2, 3)), "v"(__builtin_shufflevector(c[(i) + 1], c[(i) + 1], 0, 1)), "v"(__builtin_shufflevector(c[(i) + 1], c[(i) + 1], 2, 3))); SB(); } while (0)
#define B_E2() do { asm volatile("v_cvt_pk_bf16_f32 v216, %0, %1\n\tv_cvt_pk_bf16_f32 v217, %2, %3" :: "v"(f0.x), "v"(f0.y), "v"(f1.x), "v"(f1.y) : "v216", "v217"); SB(); } while (0)
#define B_E3(i, pad) do { asm volatile(pad "v_permlane16_swap_b32 v216, v217\n\tbuffer_store_dwordx2 v[216:217], %0, %1, %2 offen sc1" :: "v"(mso), "s"(RM), "s"((i) * 16384) : "memory", "v216", "v217"); SB(); } while (0)
        f32x2 f0, f1;
        B_MF(0, 0); B_MF(1, 0); B_MF(0, 1); B_MF(1, 1);
        B_MF(2, 0); B_MF(3, 0); B_MF(2, 1); B_MF(3, 1);
#pragma unroll
        for (int i = 4; i < 10; i += 2) {
            B_MF(i, 0); B_E1(i - 4); B_MF(i + 1, 0); B_E2(); B_MF(i, 1); B_E3(i - 4, "s_nop 0\n\t"); B_MF(i + 1, 1);
        }
        B_E1(6); B_E2(); B_E3(6, "s_nop 1\n\t");
        B_E1(8); B_E2(); B_E3(8, "s_nop 1\n\t");
        STAMPW(4);
        if (s > 0 && tid == 0) __hip_atomic_store(flags + F_M + (s - 1) * 128 + (h >> 3) * 32 + (h & 7), 1u, RLX_AGENT);
        if (s + 2 < NSEG) {
            const rsrc_t RM2 = mk(m_base(base, s + 2, selm), 655360);
#pragma unroll
            for (int i = 0; i < 3; ++i) st4(RM2, i < 2 ? mcl : mcl2, i * 65536, sent4);
        }
        STAMP_FLUSH(s);
    }
    CLK1();
    VMCNT(0);
    BAR();
    if (tid == 0) __hip_atomic_store(flags + F_M + (NSEG - 1) * 128 + (h >> 3) * 32 + (h & 7), 1u, RLX_AGENT);
    if (cap == 1u && lane == 0) __hip_atomic_store(flags + F_TMO, 1u, RLX_AGENT);
}

__device__ __forceinline__ void role_C(unsigned char* base, LAS unsigned char* L, const int cs) {
    CHAIN_SETUP();
    const int mo = cs * 16384 + w * 2048 + lane * 16, po = xp(w * 4096 + lane * 16), pmo = xp(16384 + (8 * w + 2 * g) * 128 + n * 8);
    LAS float* red = (LAS float*)L + w * 128 + 2 * g * 16 + n;
    LAS unsigned char* rp = L + ((2 * g + ((lane >> 2) & 1)) * 16 + 4 * (lane & 3) + ((lane >> 2) & 2)) * 4;
    const int opub = xp(20480 + cs * 256 + lane * 4), oclr = lane < 16 ? xp(20480 + cs * 256 + lane * 16) : OOB;
    const int obell = xp(O_BELL + w * 128), oring = (tid == 192 && cs % 10 == 0) ? xp(O_BELL + 512 + (cs / 10) * 128) : OOB;
    u32 knownU = 0, knownU1 = 0;
#pragma unroll 1
    for (int s = 0; s < NSEG - 1; ++s) {
        const int slot = (s & 3) * 65536 + sel;
        STAMP_DECL;
        if (knownU <= (u32)s) knownU = wave_min(wait_ge(flags + F_U + (32 * (s & 1) + 8 * w + (lane & 7)) * 32, (u32)s + 1u, cap, false));
        { const u32 t_ = knownU; knownU = knownU1; knownU1 = t_; }
        u32x8 uf[4];
        {
            const rsrc_t RU = mk(base + OFF_U + (size_t)s * 1310720 + (size_t)cs * 32768 + w * 8192, 8192);
#pragma unroll
            for (int t = 0; t < 4; ++t) uf[t] = cat(ld4(RU, lane * 16 + t * 2048), ld4(RU, lane * 16 + t * 2048 + 1024));
        }
        const rsrc_t RM = mk(m_base(base, s, selm), 655360);
        f32x4 c0 = Z4, c1 = Z4;
        PRE2(c0, c1);
        u32x4 m0, m1, p0, p1, p2, p3; u32x2 pa, pb;
        u32 spins = cap;
        for (;;) {
            asm volatile("" ::: "memory");
            const u32 b_ = __builtin_amdgcn_raw_buffer_load_b32(RR, obell, sel, AUX_SC1);
            if (__builtin_amdgcn_ballot_w64((int)b_ <= s) == 0) break;
            asm volatile("" : "+s"(spins));
            if (--spins == 0) break;
        }
        STAMP(4);
        cap = spins ? cap : 1u;
        spins = cap;
#pragma unroll
        for (int i = 0; i < BELL_NOPS - BELL_M; ++i) asm volatile("s_nop 7");
        asm volatile("" ::: "memory");
        m0 = ld4(RM, mo); m1 = ld4(RM, mo + 1024);
        SB();
#pragma unroll
        for (int i = 0; i < BELL_M; ++i) asm volatile("s_nop 7");
        SB();
        STAMP(5);
        p0 = ld4(RR, po, slot); p1 = ld4(RR, po + 1024, slot); p2 = ld4(RR, po + 2048, slot); p3 = ld4(RR, po + 3072, slot);
        pa = ld2(RR, pmo, slot); pb = ld2(RR, pmo + 128, slot);
        SB();
        st1(RR, oring, sel, (u32)s + 1u);
        SB();
        for (;;) {
            const u32 t_ = mx3(mx3(mx4(mx4(mx4(mx4(mx4(mx4(0u, m0), m1), p0), p1), p2), p3), pa.x, pa.y), pb.x, pb.y);
            if (__builtin_amdgcn_ballot_w64(t_ == SENT) == 0) break;
            asm volatile("" : "+s"(spins) :: "memory");
            if (--spins == 0) break;
            m0 = ld4(RM, mo); m1 = ld4(RM, mo + 1024);
            p0 = ld4(RR, po, slot); p1 = ld4(RR, po + 1024, slot); p2 = ld4(RR, po + 2048, slot); p3 = ld4(RR, po + 3072, slot);
            pa = ld2(RR, pmo, slot); pb = ld2(RR, pmo + 128, slot);
        }
        STAMP_SET(3, cap - spins);
        STAMP(6);
        const u32x8 mf = cat(m0, m1);
        const u32x4 pm = {pa.x, pa.y, pb.x, pb.y};
        SMF_V(c0, p0, uf[0], idx); SMF_V(c1, p1, uf[1], idx); SMF_V(c0, p2, uf[2], idx); SMF_V(c1, p3, uf[3], idx); SMF_V(c0, pm, mf, idx);
        POST2(c0, c1);
        const f32x2 f = fold(c0) + fold(c1);
        red[0] = f.x;
        red[16] = f.y;
        BAR();
        if (w == 0) {
            const f32x2 r0 = *(LAS f32x2*)rp, r1 = *(LAS f32x2*)(rp + 512), r2 = *(LAS f32x2*)(rp + 1024), r3 = *(LAS f32x2*)(rp + 1536);
            const f32x2 r = (r0 + r1) + (r2 + r3);
            st1(RR, opub, ((s + 1) & 3) * 65536 + sel, pk2(r));
            STAMPW(7);
            st4(RR, oclr, ((s + 3) & 3) * 65536 + sel, sent4);
        }
        BAR();
        cap = spins ? cap : 1u;
        STAMP_FLUSH(s);
    }
    if (cap == 1u && lane == 0) __hip_atomic_store(flags + F_TMO, 1u, RLX_AGENT);
}

constexpr float MASKED = -1.0e9f / 0.08838834764831845f;
constexpr int NTG = 4;
#define S_SPLIT 56
constexpr int L_XF = 0, L_EX = 81920;

template <int I> __device__ __forceinline__ void dma1(const unsigned char* sbase, u32 vo, u32 dst) {
    if constexpr ((I & 3) == 0)
        asm volatile("s_mov_b32 m0, %2\n\ts_nop 0\n\tglobal_load_lds_dwordx4 %0, %1" :: "v"(vo), "s"(sbase + I * 1024), "s"(dst + I * 1024) : "memory");
    else
        asm volatile("global_load_lds_dwordx4 %0, %1 offset:%2" :: "v"(vo), "s"(sbase + (I & ~3) * 1024), "i"((I & 3) * 1024) : "memory");
}
__device__ __forceinline__ void dma_i(int i, const unsigned char* sbase, u32 vo, u32 dst) {
    switch (i) {
        case 0: dma1<0>(sbase, vo, dst); break; case 1: dma1<1>(sbase, vo, dst); break; case 2: dma1<2>(sbase, vo, dst); break;
        case 3: dma1<3>(sbase, vo, dst); break; case 4: dma1<4>(sbase, vo, dst); break; case 5: dma1<5>(sbase, vo, dst); break;
        case 6: dma1<6>(sbase, vo, dst); break; case 7: dma1<7>(sbase, vo, dst); break; case 8: dma1<8>(sbase, vo, dst); break;
        default: dma1<9>(sbase, vo, dst); break;
    }
}
__device__ __forceinline__ void dma_xf(const unsigned char* base, int seg, int buf, int w, int lane) {
#pragma unroll
    for (int i = 0; i < 10; ++i) dma_i(i, base + OFF_XF + (size_t)seg * 40960 + w * 10240, lane * 16, L_XF + buf * 40960 + w * 10240);
}
#define PROJ(NT, acc, WT, O, xb, nsrc, ndst)                                                                             \
    do {                                                                                                                 \
        u32x4 bq[3][2];                                                                                                  \
        _Pragma("unroll") for (int i_ = 0; i_ < 2; ++i_) {                                                               \
            bq[i_][0] = *(LAS u32x4*)((xb) + i_ * 2048); bq[i_][1] = *(LAS u32x4*)((xb) + i_ * 2048 + 1024);             \
        }                                                                                                                \
        SB();                                                                                                            \
        _Pragma("unroll") for (int ks = 0; ks < 20; ++ks) {                                                              \
            if (ks + 2 < 20) {                                                                                           \
                bq[(ks + 2) % 3][0] = *(LAS u32x4*)((xb) + (ks + 2) * 2048);                                             \
                bq[(ks + 2) % 3][1] = *(LAS u32x4*)((xb) + (ks + 2) * 2048 + 1024);                                      \
            }                                                                                                            \
            SB();                                                                                                        \
            _Pragma("unroll") for (int tt = 0; tt < 2; ++tt) _Pragma("unroll") for (int t = 0; t < (NT); ++t) {          \
                if (ks == 0) {                                                                                           \
                    if ((O) + t < 64) MF0_AV(acc[tt * (NT) + t], WT[(O) + t], bq[0][tt]);                                \
                    else MF0_VV(acc[tt * (NT) + t], WT[(O) + t], bq[0][tt]);                                             \
                } else if ((O) + (NT) * ks + t < 64) MF_AV(acc[tt * (NT) + t], WT[(O) + (NT) * ks + t], bq[ks % 3][tt]); \
                else MF_VV(acc[tt * (NT) + t], WT[(O) + (NT) * ks + t], bq[ks % 3][tt]);                                 \
            }                                                                                                            \
            SB();                                                                                                        \
            if ((ks & 1) == 0) dma_i(ks >> 1, (nsrc), lane * 16, (ndst));                                                \
            SB();                                                                                                        \
        }                                                                                                                \
    } while (0)
__device__ __forceinline__ u32x4 pk8(f32x4 a, f32x4 b) { return (u32x4){pk(a[0], a[1]), pk(a[2], a[3]), pk(b[0], b[1]), pk(b[2], b[3])}; }
__device__ __forceinline__ u32x4 keyfrag(u32 a01, u32 a23, u32 b01, u32 b23) {
    auto x = __builtin_amdgcn_permlane16_swap(a01, b01, false, false);
    auto y = __builtin_amdgcn_permlane16_swap(a23, b23, false, false);
    return (u32x4){x[0], x[1], y[0], y[1]};
}
#define LOAD_2MAT(pa, pb, ld)                                                                                            \
    do {                                                                                                                 \
        const u32 vo = (lane >> 2) * (ld) + (lane & 3) * 16, to = (8 * (lane >> 4) + ((lane >> 2) & 3)) * 64 + (lane & 3) * 8; \
        const unsigned char *pa_ = (pa), *pb_ = (pb);                                                                    \
        ring_load<10>(L, w, [](int) { return 8; },                                                                       \
            [&](int u, u32 dst) { ISSUE8(pa_ + (size_t)u * 64 * (ld), pb_ + (size_t)u * 64 * (ld), ld, vo, dst); },      \
            [&](int u, LAS unsigned char* p) {                                                                           \
                _Pragma("unroll") for (int t = 0; t < 8; ++t)                                                            \
                    wt[u * 8 + t] = frag(p + to + (t >> 2) * 2048 + ((t >> 1) & 1) * 4096 + (t & 1) * 32, 256); });      \
    } while (0)
#define STREAM_SETUP()                                                                                                   \
    const int tid = threadIdx.x, lane = tid & 63, w = __builtin_amdgcn_readfirstlane(tid >> 6);                          \
    GAS u32* flags = (GAS u32*)(base + OFF_FLAG);                                                                        \
    const unsigned char* const xsrc = base + OFF_XF + w * 10240

__device__ __forceinline__ void role_D(unsigned char* base, LAS unsigned char* L, const Wts& W, const int h, const int par) {
    STREAM_SETUP();
    dma_xf(base, par, 0, w, lane);
    u32x4 wt[80];
    {
        const int n = lane & 15, g = lane >> 4;
        const u32 vo = (lane >> 2) * 8192 + (lane & 3) * 16, to = (8 * g + (n >> 2)) * 64 + (n & 3) * 8, tp = (4 * g + (n >> 2)) * 320 + (n & 3) * 8;
        const size_t co = h * 256 + w * 64;
        WO_SETUP();
        ring_load<9>(L, w, [](int u) { return u < 5 ? 8 : 10; },
            [&](int u, u32 dst) {
                if (u < 5) ISSUE8(W.v + co + (size_t)u * 128 * 8192, W.v + co + ((size_t)u * 128 + 64) * 8192, 8192, vo, dst);
                else ISSUE_WO(u - 5, dst); },
            [&](int u, LAS unsigned char* p) {
                if (u < 5) {
#pragma unroll
                    for (int t = 0; t < 8; ++t) wt[u * 8 + t] = frag(p + to + (t >> 1) * 2048 + (t & 1) * 32, 256);
                } else {
#pragma unroll
                    for (int i = 0; i < 10; ++i) wt[40 + 4 * i + (u - 5)] = frag(p + tp + i * 32, 16 * 320);
                } });
    }
    VMCNT(0);
    BAR();
    MARK(1);
#pragma unroll 1
    for (int s = par; s < NSEG; s += 2) {
        STAMP_DECL;
        STAMPW(0);
        f32x4 acc[4];
        LAS unsigned char* xb = L + L_XF + ((s >> 1) & 1) * 40960 + lane * 16;
        PROJ(2, acc, wt, 0, xb, xsrc + (size_t)((s + 2) & (NSEG - 1)) * 40960, L_XF + (((s >> 1) + 1) & 1) * 40960 + w * 10240);
        POST4(acc[0], acc[1], acc[2], acc[3]);
        STAMPW(1);
        *(LAS u32x4*)(L + L_EX + w * 1024 + lane * 16) = pk8(acc[0], acc[1]);
        *(LAS u32x4*)(L + L_EX + 4096 + w * 1024 + lane * 16) = pk8(acc[2], acc[3]);
        BAR();
        STAMPW(2);
        u32x4 vf[8];
#pragma unroll
        for (int i = 0; i < 4; ++i) { vf[i] = *(LAS u32x4*)(L + L_EX + i * 1024 + lane * 16); vf[4 + i] = *(LAS u32x4*)(L + L_EX + 4096 + i * 1024 + lane * 16); SB(); }
        const rsrc_t RU = mk(base + OFF_U + (size_t)s * 1310720, 1310720);
        f32x4 d[10][2];
        u32 e0 = 0, e1 = 0, e2 = 0, e3 = 0;
#pragma unroll
        for (int i = 0; i < 12; ++i) {
            if (i == 10) { SB(); asm volatile("s_nop 7"); }
#pragma unroll
            for (int e = 0; e < 4; ++e) {
                if (i < 10) {
                    if (e == 0) {
                        if (40 + 4 * i < 64) { MF0_VA(d[i][0], vf[0], wt[40 + 4 * i]); MF0_VA(d[i][1], vf[4], wt[40 + 4 * i]); }
                        else { MF0_VV(d[i][0], vf[0], wt[40 + 4 * i]); MF0_VV(d[i][1], vf[4], wt[40 + 4 * i]); }
                    } else if (40 + 4 * i + e < 64) { MF_VA(d[i][0], vf[e], wt[40 + 4 * i + e]); MF_VA(d[i][1], vf[4 + e], wt[40 + 4 * i + e]); }
                    else { MF_VV(d[i][0], vf[e], wt[40 + 4 * i + e]); MF_VV(d[i][1], vf[4 + e], wt[40 + 4 * i + e]); }
                }
                SB();
                if (i >= 2) {
                    if (e == 0) { e0 = pk(d[i - 2][0][0], d[i - 2][0][1]); e1 = pk(d[i - 2][0][2], d[i - 2][0][3]); }
                    if (e == 1) { e2 = pk(d[i - 2][1][0], d[i - 2][1][1]); e3 = pk(d[i - 2][1][2], d[i - 2][1][3]); }
                    if (e == 2) st4(RU, lane * 16, (10 * w + i - 2) * 32768 + h * 1024, keyfrag(e0, e1, e2, e3));
                }
                SB();
            }
        }
        STAMPW(3);
        if (s < 4) VMCNT(0); else VMCNT(10);
        STAMPW(4);
        BAR();
        if (tid == 0) __hip_atomic_store(flags + F_U + (32 * par + h) * 32, (u32)(s < 4 ? s + 1 : s - 1), RLX_AGENT);
        STAMP_FLUSH(s);
    }
    VMCNT(0);
    BAR();
    if (tid == 0) __hip_atomic_store(flags + F_U + (32 * par + h) * 32, (u32)NSEG, RLX_AGENT);
}

__device__ __forceinline__ void role_E(unsigned char* base, LAS unsigned char* L, const Wts& W, const int h) {
    STREAM_SETUP();
    dma_xf(base, 0, 0, w, lane);
    u32x4 wt[80];
    {
        const u32 vo = (lane >> 3) * 8192 + (lane & 7) * 16, to = (8 * (lane >> 4) + ((lane >> 2) & 3)) * 128 + (lane & 3) * 8;
        const unsigned char* src = (w < 2 ? W.k : W.q) + h * 256 + (w & 1) * 128;
        ring_load<10>(L, w, [](int) { return 8; },
            [&](int u, u32 dst) { ISSUE8W(src + (size_t)u * 64 * 8192, 8192, vo, dst); },
            [&](int u, LAS unsigned char* p) {
#pragma unroll
                for (int t = 0; t < 8; ++t) wt[u * 8 + t] = frag(p + to + (t >> 2) * 4096 + (t & 3) * 32, 512); });
    }
    VMCNT(0);
    BAR();
    MARK(1);
#pragma unroll 1
    for (int s = 0; s < NSEG; ++s) {
        STAMP_DECL;
        STAMPW(0);
        f32x4 acc[8];
        LAS unsigned char* xb = L + L_XF + (s & 1) * 40960 + lane * 16;
        PROJ(4, acc, wt, 0, xb, xsrc + (size_t)((s + 1) & (NSEG - 1)) * 40960, L_XF + ((s + 1) & 1) * 40960 + w * 10240);
        asm volatile("" : "+v"(acc[0]), "+v"(acc[1]), "+v"(acc[2]), "+v"(acc[3]));
        POST4(acc[4], acc[5], acc[6], acc[7]);
        STAMPW(1);
        const rsrc_t RK = mk(base + OFF_KX + ((size_t)s * NH + h) * 16384, 16384);
        const int ko = (w >> 1) * 8192 + (w & 1) * 2048;
        st4(RK, lane * 16, ko, pk8(acc[0], acc[1]));
        st4(RK, lane * 16, ko + 1024, pk8(acc[2], acc[3]));
        st4(RK, lane * 16, ko + 4096, pk8(acc[4], acc[5]));
        st4(RK, lane * 16, ko + 5120, pk8(acc[6], acc[7]));
        STAMPW(2);
        if (s < 6) VMCNT(0); else VMCNT(4);
        STAMPW(3);
        BAR();
        if (tid == 0) __hip_atomic_store(flags + F_K + h * 32, (u32)(s < 6 ? s + 1 : s), RLX_AGENT);
        STAMPW(4);
        STAMP_FLUSH(s);
    }
    VMCNT(0);
    BAR();
    if (tid == 0) __hip_atomic_store(flags + F_K + h * 32, (u32)NSEG, RLX_AGENT);
}

__device__ __forceinline__ void role_P(unsigned char* base, const int i) {
    const int tid = threadIdx.x, lane = tid & 63, w = __builtin_amdgcn_readfirstlane(tid >> 6);
    const int n = lane & 15, g = lane >> 4, h = i * 2 + (w >> 1), t = w & 1;
    GAS u32* flags = (GAS u32*)(base + OFF_FLAG);
    u32 cap = SPIN_CAP, knownK, knownM = 0;
    const int kmo = n >= 8 ? (g * 8 + n - 8) * 16 : OOB, tok = 16 * t + n;
    u32x4 kx[8], qf[4], km[4];
    knownK = (u32)__builtin_amdgcn_readfirstlane((int)wait_ge(flags + F_K + h * 32, 1u, cap, true));
    {
        const rsrc_t RK = mk(base + OFF_KX + (size_t)h * 16384, 16384);
#pragma unroll
        for (int e = 0; e < 4; ++e) { kx[e] = ld4(RK, lane * 16, e * 1024); kx[4 + e] = ld4(RK, lane * 16, 4096 + e * 1024); qf[e] = ld4(RK, lane * 16, 8192 + t * 4096 + e * 1024); }
    }
#pragma unroll 1
    for (int s = 0; s < NSEG; ++s) {
        STAMP_DECL;
        STAMPW(0);
        const rsrc_t RKM = mk(base + OFF_KM + ((size_t)s * NH + h) * 2048, 2048);
        const bool early = knownM > (u32)s;
        if (early) {
#pragma unroll
            for (int e = 0; e < 4; ++e) km[e] = ld4(RKM, kmo, e * 512);
        }
        f32x4 t0, t1, t2;
        MF0_VV(t1, kx[0], qf[0]); MF0_VV(t2, kx[4], qf[0]);
#pragma unroll
        for (int e = 1; e < 4; ++e) { MF_VV(t1, kx[e], qf[e]); MF_VV(t2, kx[4 + e], qf[e]); }
        POST2(t1, t2);
        float y[12];
#pragma unroll
        for (int r = 0; r < 4; ++r) {
            y[r] = 4 * g + r <= tok ? t1[r] : MASKED;
            y[4 + r] = 16 + 4 * g + r <= tok ? t2[r] : MASKED;
        }
        const float mxx = fmax3(fmax3(y[0], y[1], y[2]), fmax3(y[3], y[4], y[5]), fmax3(y[6], y[7], y[7]));
        STAMPW(1);
        if (!early) {
            knownM = (u32)__builtin_amdgcn_readfirstlane((int)wait_ge(flags + F_KM + h * 32, (u32)s + 1u, cap, false));
#pragma unroll
            for (int e = 0; e < 4; ++e) km[e] = ld4(RKM, kmo, e * 512);
        }
        STAMPW(2);
        MF0_VV(t0, km[0], qf[0]);
#pragma unroll
        for (int e = 1; e < 4; ++e) MF_VV(t0, km[e], qf[e]);
        asm volatile("s_nop 7" : "+v"(t0));
#pragma unroll
        for (int r = 0; r < 4; ++r) y[8 + r] = g >= 2 ? t0[r] : -INFINITY;
        float mx = fmax3(fmax3(mxx, y[8], y[9]), y[10], y[11]);
        mx = max16(mx); mx = max32(mx);
        const float cf = 0.08838834764831845f * 1.4426950408889634f;
        float sum = 0.f;
#pragma unroll
        for (int r = 0; r < 12; ++r) { y[r] = __builtin_amdgcn_exp2f((y[r] - mx) * cf); sum += y[r]; }
        sum = add16(sum); sum = add32(sum);
        const float rs = __builtin_amdgcn_rcpf(sum);
        const rsrc_t RP = mk(base + OFF_PX + (size_t)s * 65536 + t * 32768 + h * 1024, 1024);
        st4(RP, lane * 16, 0, keyfrag(pk(y[0] * rs, y[1] * rs), pk(y[2] * rs, y[3] * rs), pk(y[4] * rs, y[5] * rs), pk(y[6] * rs, y[7] * rs)));
        const rsrc_t RQ = mk(base + OFF_PM + (size_t)s * 16384 + t * 8192 + h * 256, 256);
        st2(RQ, g >= 2 ? n * 16 + (g - 2) * 8 : OOB, 0, (u32x2){pk(y[8] * rs, y[9] * rs), pk(y[10] * rs, y[11] * rs)});
        STAMPW(3);
        if (s + 1 < NSEG) {
            if (knownK <= (u32)s + 1u) knownK = (u32)__builtin_amdgcn_readfirstlane((int)wait_ge(flags + F_K + h * 32, (u32)s + 2u, cap, true));
            const rsrc_t RK = mk(base + OFF_KX + ((size_t)(s + 1) * NH + h) * 16384, 16384);
#pragma unroll
            for (int e = 0; e < 4; ++e) { kx[e] = ld4(RK, lane * 16, e * 1024); kx[4 + e] = ld4(RK, lane * 16, 4096 + e * 1024); qf[e] = ld4(RK, lane * 16, 8192 + t * 4096 + e * 1024); }
            u32 pm_;
            asm volatile("global_load_dword %0, %1, off sc1" : "=v"(pm_) : "v"(flags + F_KM + h * 32) : "memory");
            VMCNT(13);
            if (lane == 0) __hip_atomic_store(flags + F_PX + s * 128 + (h >> 3) * 32 + 8 * t + (h & 7), 1u, RLX_AGENT);
            asm volatile("s_waitcnt vmcnt(1)" : "+v"(pm_) :: "memory");
            knownM = (u32)__builtin_amdgcn_readfirstlane((int)pm_);
            __builtin_amdgcn_fence(__ATOMIC_ACQUIRE, "wavefront");
        } else {
            VMCNT(0);
            if (lane == 0) __hip_atomic_store(flags + F_PX + s * 128 + (h >> 3) * 32 + 8 * t + (h & 7), 1u, RLX_AGENT);
        }
        STAMPW(4);
        STAMP_FLUSH(s);
    }
    if (cap == 1u && lane == 0) __hip_atomic_store(flags + F_TMO, 1u, RLX_AGENT);
}

__device__ __forceinline__ void role_G(unsigned char* base, LAS unsigned char* L, const Wts& W, const int i) {
    STREAM_SETUP();
    const int set = i % 14, tg = i / 14;
    const int fs = 4 * set + w, fsc = fs < 54 ? fs : 53;
    dma_xf(base, tg, 0, w, lane);
    u32x4 wt[80];
    LOAD_2MAT(W.g + fsc * 64, W.u + fsc * 64, 3456);
    VMCNT(0);
    BAR();
    int it = 0;
#pragma unroll 1
    for (int s = tg; s < NSEG; s += NTG, ++it) {
        f32x4 acc[8];
        LAS unsigned char* xb = L + L_XF + (it & 1) * 40960 + lane * 16;
        PROJ(4, acc, wt, 0, xb, xsrc + (size_t)(s + NTG < NSEG ? s + NTG : 0) * 40960, L_XF + ((it + 1) & 1) * 40960 + w * 10240);
        asm volatile("" : "+v"(acc[0]), "+v"(acc[1]), "+v"(acc[2]), "+v"(acc[3]));
        POST4(acc[4], acc[5], acc[6], acc[7]);
#pragma unroll
        for (int q = 0; q < 4; ++q)
#pragma unroll
            for (int r = 0; r < 4; ++r) {
                f32x4& ga = acc[(q >> 1) * 4 + (q & 1)];
                const float gt = rbf(ga[r]);
                ga[r] = (gt * (gt > 0.f ? 1.f : 0.f)) * rbf(acc[(q >> 1) * 4 + (q & 1) + 2][r]);
            }
        const rsrc_t RH = mk(base + OFF_HF + (size_t)s * 110592, 110592);
        st4(RH, fs < 54 ? lane * 16 : OOB, fsc * 1024, pk8(acc[0], acc[1]));
        st4(RH, fs < 54 ? lane * 16 : OOB, 55296 + fsc * 1024, pk8(acc[4], acc[5]));
        VMCNT(2);
        BAR();
        if (tid == 0 && it > 0) __hip_atomic_fetch_add(flags + F_H + s - NTG, 1u, RLX_AGENT);
    }
    VMCNT(0);
    BAR();
    if (tid == 0) __hip_atomic_fetch_add(flags + F_H + tg + NTG * (it - 1), 1u, RLX_AGENT);
    MARK(2);
}

__device__ __forceinline__ void role_F(unsigned char* base, LAS unsigned char* L, const Wts& W, const int i, const u32* __restrict__ x, u32* __restrict__ out) {
    const int tid = threadIdx.x, lane = tid & 63, w = __builtin_amdgcn_readfirstlane(tid >> 6);
    const int n = lane & 15, g = lane >> 4;
    GAS u32* flags = (GAS u32*)(base + OFF_FLAG);
    u32 cap = SPIN_CAP;
    const int cg = i % 10;
    u32x4 wd[56];
    {
        const u32 vo = (lane >> 3) * 1280 + (lane & 7) * 16, tp = (4 * g + (n >> 2)) * 128 + (n & 3) * 8;
        ring_load<7>(L, w, [](int) { return 8; },
            [&](int u, u32 dst) {
#pragma unroll
                for (int k = 0; k < 2; ++k) {
                    const int fs = min(14 * w + 2 * u + k, 53);
#pragma unroll
                    for (int q = 0; q < 4; ++q) dmag(W.d + ((size_t)fs * 32 + 8 * q) * 1280 + cg * 128, vo, dst + k * 4096 + q * 1024);
                } },
            [&](int u, LAS unsigned char* p) {
#pragma unroll
                for (int t = 0; t < 8; ++t) wd[u * 8 + t] = frag(p + tp + (t >> 2) * 4096 + (t & 3) * 32, 2048); });
        BAR();
    }
    const int ocol = (cg * 4 + w) * 8 + g * 2;
    u64 okH = 0, okP = 0;
    u32x4 hb[28], pa[2];
    u32x2 x0, x1;
#define F_FETCH(s_)                                                                                                      \
    do {                                                                                                                 \
        if (!((okH >> (s_)) & 1)) { wait_ge(flags + F_H + (s_), 14u, cap, true); okH = __builtin_amdgcn_ballot_w64(__hip_atomic_load(flags + F_H + lane, RLX_AGENT) >= 14u); } \
        if ((s_) < S_SPLIT && !((okP >> (s_)) & 1)) {                                                                    \
            wait_ge(flags + F_PT + (s_) * 32 + cg * 2 + (w >> 1), 1u, cap, true);                                        \
            okP = __builtin_amdgcn_ballot_w64(__hip_atomic_load(flags + F_PT + lane * 32 + cg * 2 + (w >> 1), RLX_AGENT) != 0u); \
        }                                                                                                                \
        __builtin_amdgcn_fence(__ATOMIC_ACQUIRE, "wavefront");                                                           \
        const rsrc_t RH = mk(base + OFF_HF + (size_t)(s_) * 110592, 110592);                                             \
        const rsrc_t RT_ = mk(base + OFF_PT + (size_t)(s_) * 81920 + (cg * 4 + w) * 2048, 2048);                         \
        _Pragma("unroll") for (int k = 0; k < 14; ++k) {                                                                 \
            const int vo = 14 * w + k < 54 ? lane * 16 : OOB;                                                            \
            hb[2 * k] = ld4(RH, vo, (14 * w + k) * 1024); hb[2 * k + 1] = ld4(RH, vo, 55296 + (14 * w + k) * 1024);      \
        }                                                                                                                \
        pa[0] = ld4(RT_, (s_) < S_SPLIT ? lane * 16 : OOB); pa[1] = ld4(RT_, (s_) < S_SPLIT ? lane * 16 : OOB, 1024);    \
        x0 = *(const u32x2*)(x + ((s_) * 32 + n) * (DM / 2) + ocol); x1 = *(const u32x2*)(x + ((s_) * 32 + 16 + n) * (DM / 2) + ocol); \
    } while (0)
    F_FETCH((i / 10 + S_SPLIT) & (NSEG - 1));
#pragma unroll 1
    for (int k0 = i / 10; k0 < NSEG; k0 += 6) {
        const int s = (k0 + S_SPLIT) & (NSEG - 1);
        STAMP_DECL;
        STAMPW(5);
        f32x4 acc[8];
#pragma unroll
        for (int k = 0; k < 14; ++k)
#pragma unroll
            for (int q = 0; q < 8; ++q) { if (k == 0) MF0_AV(acc[q], wd[q >> 1], hb[q & 1]); else MF_AV(acc[q], wd[4 * k + (q >> 1)], hb[2 * k + (q & 1)]); }
        f32x4 r0 = (f32x4){bflo(x0.x), bfhi(x0.x), bflo(x0.y), bfhi(x0.y)} + __builtin_bit_cast(f32x4, pa[0]);
        f32x4 r1 = (f32x4){bflo(x1.x), bfhi(x1.x), bflo(x1.y), bfhi(x1.y)} + __builtin_bit_cast(f32x4, pa[1]);
        asm volatile("" : "+v"(acc[0]), "+v"(acc[1]), "+v"(acc[2]), "+v"(acc[3]), "+v"(r0), "+v"(r1));
        POST4(acc[4], acc[5], acc[6], acc[7]);
#pragma unroll
        for (int q = 0; q < 8; ++q) *(LAS f32x4*)(L + (w * 8 + q) * 1024 + lane * 16) = acc[q];
        SB();
        if (s < S_SPLIT && k0 + 6 < NSEG) F_FETCH((k0 + 6 + S_SPLIT) & (NSEG - 1));
        BAR();
#pragma unroll
        for (int k = 0; k < 4; ++k) { r0 += *(LAS f32x4*)(L + (k * 8 + 2 * w) * 1024 + lane * 16); r1 += *(LAS f32x4*)(L + (k * 8 + 2 * w + 1) * 1024 + lane * 16); }
        if (s < S_SPLIT) {
            *(u32x2*)(out + (s * 32 + n) * (DM / 2) + ocol) = (u32x2){pk(r0[0], r0[1]), pk(r0[2], r0[3])};
            *(u32x2*)(out + (s * 32 + 16 + n) * (DM / 2) + ocol) = (u32x2){pk(r1[0], r1[1]), pk(r1[2], r1[3])};
            BAR();
        } else {
            const rsrc_t RT = mk(base + OFF_PT + (size_t)s * 81920 + (cg * 4 + w) * 2048, 2048);
            st4(RT, lane * 16, 0, __builtin_bit_cast(u32x4, r0)); st4(RT, lane * 16, 1024, __builtin_bit_cast(u32x4, r1));
            VMCNT(0);
            BAR();
            if (tid < 2) __hip_atomic_store(flags + F_PT + s * 32 + cg * 2 + tid, 1u, RLX_AGENT);
            F_FETCH((k0 + 6 + S_SPLIT) & (NSEG - 1));
        }
        STAMPW(6);
        STAMP_FLUSH2(s);
    }
    if (cap == 1u && lane == 0) __hip_atomic_store(flags + F_TMO, 1u, RLX_AGENT);
}

__device__ __forceinline__ void role_O(unsigned char* base, LAS unsigned char* L, const int i, u32* __restrict__ out) {
    const int tid = threadIdx.x, lane = tid & 63, w = __builtin_amdgcn_readfirstlane(tid >> 6);
    const int n = lane & 15, g = lane >> 4;
    GAS u32* flags = (GAS u32*)(base + OFF_FLAG);
    u32 cap = SPIN_CAP;
    const u64 selm = sel_mask(flags, lane);
    const int cg = i % 20;
    const int ocol = (cg * 2 + (w >> 1)) * 8 + g * 2, otok = (w & 1) * 16 + n;
#pragma unroll 1
    for (int s = i / 20; s < NSEG; s += 2) {
        STAMP_DECL;
        STAMPW(0);
        if (s < 2) wait_ge(flags + F_M + w * 32, 1u, cap, true);
        wait_ge(flags + F_U + (32 * (s & 1) + 8 * w + (lane & 7)) * 32, (u32)s + 1u, cap, true);
        u32x4 uf[16], mf[4];
        {
            const rsrc_t RU = mk(base + OFF_U + (size_t)s * 1310720 + (size_t)cg * 2 * 32768, 2 * 32768);
#pragma unroll
            for (int k = 0; k < 8; ++k)
#pragma unroll
                for (int c = 0; c < 2; ++c) uf[2 * k + c] = ld4(RU, lane * 16, c * 32768 + (8 * w + k) * 1024);
        }
        const rsrc_t RT = mk(base + OFF_PT + (size_t)s * 81920 + (cg * 4 + w) * 1024, 1024);
        f32x4 r = Z4;
        if (s >= S_SPLIT) {
            wait_ge(flags + F_PT + s * 32 + cg, 1u, cap, true);
            r = __builtin_bit_cast(f32x4, ld4(RT, lane * 16));
        }
        wait_ge(flags + F_M + s * 128 + w * 32 + (lane & 7), 1u, cap, true);
        STAMPW(1);
        {
            const rsrc_t RM = mk(m_base(base, s, selm) + (size_t)cg * 2 * 16384, 2 * 16384);
#pragma unroll
            for (int k = 0; k < 2; ++k)
#pragma unroll
                for (int c = 0; c < 2; ++c) mf[2 * k + c] = ld4(RM, lane * 16, c * 16384 + (2 * w + k) * 1024);
        }
        wait_ge(flags + F_PX + s * 128 + w * 32 + (lane & 15), 1u, cap, false);
        STAMPW(2);
        f32x4 acc[4];
        {
            const rsrc_t RP = mk(base + OFF_PX + (size_t)s * 65536, 65536);
            const rsrc_t RQ = mk(base + OFF_PM + (size_t)s * 16384, 16384);
            u32x4 pf[16], pm[4];
#pragma unroll
            for (int k = 0; k < 8; ++k) { pf[2 * k] = ld4(RP, lane * 16, (8 * w + k) * 1024); pf[2 * k + 1] = ld4(RP, lane * 16, 32768 + (8 * w + k) * 1024); }
#pragma unroll
            for (int k = 0; k < 2; ++k) { pm[2 * k] = ld4(RQ, lane * 16, (2 * w + k) * 1024); pm[2 * k + 1] = ld4(RQ, lane * 16, 8192 + (2 * w + k) * 1024); }
#pragma unroll
            for (int k = 0; k < 8; ++k)
#pragma unroll
                for (int q = 0; q < 4; ++q) { if (k == 0) MF0_VV(acc[q], uf[q >> 1], pf[q & 1]); else MF_VV(acc[q], uf[2 * k + (q >> 1)], pf[2 * k + (q & 1)]); }
#pragma unroll
            for (int k = 0; k < 2; ++k)
#pragma unroll
                for (int q = 0; q < 4; ++q) MF_VV(acc[q], mf[2 * k + (q >> 1)], pm[2 * k + (q & 1)]);
        }
        POST4(acc[0], acc[1], acc[2], acc[3]);
        STAMPW(3);
#pragma unroll
        for (int q = 0; q < 4; ++q) *(LAS f32x4*)(L + (w * 4 + q) * 1024 + lane * 16) = acc[q];
        BAR();
#pragma unroll
        for (int k = 0; k < 4; ++k) r += *(LAS f32x4*)(L + (k * 4 + w) * 1024 + lane * 16);
        if (s >= S_SPLIT) {
            *(u32x2*)(out + (s * 32 + otok) * (DM / 2) + ocol) = (u32x2){pk(r[0], r[1]), pk(r[2], r[3])};
            BAR();
        } else {
            st4(RT, lane * 16, 0, __builtin_bit_cast(u32x4, r));
            VMCNT(0);
            BAR();
            if (tid == 0) __hip_atomic_store(flags + F_PT + s * 32 + cg, 1u, RLX_AGENT);
        }
        STAMPW(4);
        STAMP_FLUSH(s);
    }
    if (cap == 1u && lane == 0) __hip_atomic_store(flags + F_TMO, 1u, RLX_AGENT);
}

constexpr int LDS_MAIN = 163840;
enum { M_CHAIN = 1, M_D = 2, M_E1 = 4, M_G = 8, M_E2 = 16, M_O = 32, M_ALL = 63 };

__global__ void __launch_bounds__(256, 1) k_main(const u32* __restrict__ x, u32* __restrict__ out, unsigned char* __restrict__ base, const unsigned char* wq, const unsigned char* wk,
                                                 const unsigned char* wv, const unsigned char* wo, const unsigned char* wg, const unsigned char* wu, const unsigned char* wd, int mask) {
    const Wts W = {wq, wk, wv, wo, wg, wu, wd};
    extern __shared__ __attribute__((aligned(16))) unsigned char lds[];
    LAS unsigned char* L = (LAS unsigned char*)lds;
    const int xg = blockIdx.x & 7, j = blockIdx.x >> 3;
    MARK(0);
    int role, arg;
    if (xg == 1) { role = 0; arg = j; }
    else if (xg == 3) { role = 1; arg = j; }
    else if (xg < 4) {
        if (j < 20) { role = 2; arg = j + 10 * xg; }
        else if (j < 28) { role = 5; arg = j - 20 + 4 * xg; }
        else { role = 6; arg = 32 + j - 28 + 2 * xg; }
    } else {
        const int i = j * 4 + xg - 4;
        if (i < 64) { role = 3; arg = i; }
        else if (i < 96) { role = 4; arg = i - 64; }
        else { role = 6; arg = i - 96; }
    }
    if (role == 0) { if (mask & M_CHAIN) role_A(base, L, W, arg); }
    else if (role == 1) { if (mask & M_CHAIN) role_B(base, L, W, arg); }
    else if (role == 2) { if (mask & M_CHAIN) role_C(base, L, arg); }
    else if (role == 3) {
        if (mask & M_D) role_D(base, L, W, arg & 31, arg >> 5);
        if ((mask & M_G) && arg < 14 * NTG) role_G(base, L, W, arg);
        if ((mask & M_O) && arg < 60) role_F(base, L, W, arg, x, out);
    }
    else if (role == 4) { if (mask & M_E1) role_E(base, L, W, arg); }
    else if (role == 5) { if (mask & M_E2) role_P(base, arg); }
    else if (mask & M_O) role_O(base, L, arg, out);
    MARK(3);
}

#define SPLIT_LAUNCH 0

extern "C" void kernel_launch(void* const* d_in, const int* in_sizes, int n_in, void* d_out, int out_size, void* d_ws, size_t ws_size, hipStream_t stream) {
    (void)in_sizes; (void)n_in; (void)out_size; (void)ws_size;
    unsigned char* base = (unsigned char*)(((size_t)d_ws + (2u << 20) - 1) & ~(size_t)((2u << 20) - 1));
    (void)hipFuncSetAttribute((const void*)k_main, hipFuncAttributeMaxDynamicSharedMemorySize, LDS_MAIN);
    k_prep<<<PB_ALL, 256, 0, stream>>>((const u32*)d_in[0], (const u32*)d_in[1], base);
#define MAIN(mask) k_main<<<256, 256, LDS_MAIN, stream>>>((const u32*)d_in[0], (u32*)d_out, base, (const unsigned char*)d_in[2], (const unsigned char*)d_in[3], \
        (const unsigned char*)d_in[4], (const unsigned char*)d_in[5], (const unsigned char*)d_in[8], (const unsigned char*)d_in[9], (const unsigned char*)d_in[10], mask)
    MAIN(M_ALL);
}
```

```cpp
#include <hip/hip_runtime.h>
#include <hip/hip_bf16.h>

typedef unsigned u32;
typedef unsigned long long u64;
typedef u32 u32x2 __attribute__((ext_vector_type(2)));
typedef u32 u32x4 __attribute__((ext_vector_type(4)));
typedef u32 u32x8 __attribute__((ext_vector_type(8)));
typedef float f32x2 __attribute__((ext_vector_type(2)));
typedef float f32x4 __attribute__((ext_vector_type(4)));
typedef short s16x4 __attribute__((ext_vector_type(4)));
typedef __bf16 bf16x2 __attribute__((ext_vector_type(2)));
#define LAS __attribute__((address_space(3)))
#define GAS __attribute__((address_space(1)))
#define RLX_AGENT __ATOMIC_RELAXED, __HIP_MEMORY_SCOPE_AGENT
typedef __amdgpu_buffer_rsrc_t rsrc_t;

#define STAMPS 0

constexpr int NSEG = 64, DM = 640, NH = 32, FF = 1728;
constexpr u32 SENT = 0xFFFFFFFFu;
constexpr int OOB = 0x7F000000;
constexpr int AUX_SC1 = 16;
constexpr u32 SPIN_CAP = 400000;

constexpr size_t MB2 = 2u << 20;
constexpr size_t OFF_RING = 0;
constexpr int    SLOT     = 131072;
constexpr size_t OFF_FLAG = 4 * SLOT;
constexpr size_t OFF_DBG  = OFF_FLAG + 131072;
constexpr size_t OFF_HS   = 5u << 18;
constexpr size_t OFF_CAL  = 3 * 655360;
constexpr int    NMBLK    = 22;
constexpr size_t OFF_MB   = MB2;
constexpr size_t OFF_XF   = OFF_MB + NMBLK * MB2;
constexpr size_t OFF_KX   = OFF_XF + (size_t)NSEG * 40960;
constexpr size_t OFF_KM   = OFF_KX + (size_t)NSEG * NH * 16384;
constexpr size_t OFF_PX   = OFF_KM + (size_t)NSEG * NH * 2048;
constexpr size_t OFF_PM   = OFF_PX + (size_t)NSEG * 65536;
constexpr size_t OFF_HF   = OFF_PM + (size_t)NSEG * 16384;
constexpr size_t OFF_PT   = OFF_HF + (size_t)NSEG * 110592;
constexpr size_t OFF_U    = OFF_PT + (size_t)NSEG * 81920;
constexpr size_t WS_END   = OFF_U + (size_t)NSEG * 1310720;
static_assert(WS_END + MB2 <= (256u << 20), "workspace");
static_assert(OFF_DBG + 524288 + 8192 <= OFF_HS && OFF_CAL + 16384 <= MB2, "block 0");
constexpr int F_H = 320  , F_SEL = 448  , F_TMO = 512;
constexpr int F_KM = 1024;
constexpr int F_M = 2048;
constexpr int F_PX = F_M + 8192;
constexpr int F_PT = F_PX + 8192;
constexpr int F_K = F_PT + 2048;
constexpr int F_U = F_K + 1024;
constexpr int F_WORDS = F_U + 2048;
static_assert(F_WORDS * 4 <= 98304, "flags");
#define D_TWIN 24
static_assert(D_TWIN >= 10 && D_TWIN % 2 == 0, "twins");
constexpr int O_BELL = 30720;
#define T_P 80
#define T_A 550
#define T_B 40
#define P_REP 1
#ifndef M_NOW
#define M_NOW 61
#endif
#define AB_LATE 0
#define U_EARLY 4
#define T_M 40
#define T_BELL 600
constexpr int C_P = 1650, C_A = 350;
#define CAL_N 4
#define T_UP 30
#define N_UP 5
#define N_DN 8

__device__ __forceinline__ int xp(int o) { return ((o >> 13) << 14) | (o & 0x1fff); }
__device__ __forceinline__ rsrc_t mk(const void* p, u32 bytes) { return __builtin_amdgcn_make_buffer_rsrc((void*)p, 0, bytes, 0x00020000); }
__device__ __forceinline__ u32x4 ld4(rsrc_t r, int off, int so = 0) { return __builtin_amdgcn_raw_buffer_load_b128(r, off, so, AUX_SC1); }
__device__ __forceinline__ u32x2 ld2(rsrc_t r, int off, int so = 0) { return __builtin_amdgcn_raw_buffer_load_b64(r, off, so, AUX_SC1); }
__device__ __forceinline__ u32x4 ld4p(rsrc_t r, int off, int so = 0) { return __builtin_amdgcn_raw_buffer_load_b128(r, off, so, 0); }
__device__ __forceinline__ void st4(rsrc_t r, int off, int so, u32x4 v) { __builtin_amdgcn_raw_buffer_store_b128(v, r, off, so, AUX_SC1); }
__device__ __forceinline__ void st2(rsrc_t r, int off, int so, u32x2 v) { __builtin_amdgcn_raw_buffer_store_b64(v, r, off, so, AUX_SC1); }
__device__ __forceinline__ void st1(rsrc_t r, int off, int so, u32 v) { __builtin_amdgcn_raw_buffer_store_b32(v, r, off, so, AUX_SC1); }
#define OUT_AUX 0
__device__ __forceinline__ void st_out(u32* out, int word, u32x2 v) { __builtin_amdgcn_raw_buffer_store_b64(v, mk(out, 2048 * DM * 2), word * 4, 0, OUT_AUX); }
__device__ __forceinline__ u32 pk(float a, float b) { f32x2 f = {a, b}; return __builtin_bit_cast(u32, __builtin_convertvector(f, bf16x2)); }
__device__ __forceinline__ u32 pks(float a, float b) { u32 v = pk(a, b); return v < 0xFFFFFFFEu ? v : 0xFFFFFFFEu; }
__device__ __forceinline__ float bflo(u32 v) { return __builtin_bit_cast(float, v << 16); }
__device__ __forceinline__ float bfhi(u32 v) { return __builtin_bit_cast(float, v & 0xffff0000u); }
__device__ __forceinline__ float rbf(float a) { return bflo(pk(a, a) & 0xffffu); }
__device__ __forceinline__ u32x8 cat(u32x4 a, u32x4 b) { return __builtin_shufflevector(a, b, 0, 1, 2, 3, 4, 5, 6, 7); }
__device__ __forceinline__ bool ok4(u32x4 v) { return (v.x != SENT) & (v.y != SENT) & (v.z != SENT) & (v.w != SENT); }
__device__ __forceinline__ bool ok2(u32x2 v) { return (v.x != SENT) & (v.y != SENT); }
__device__ __forceinline__ u32x2 trd(LAS unsigned char* p) { return __builtin_bit_cast(u32x2, __builtin_amdgcn_ds_read_tr16_b64_v4i16((LAS s16x4*)p)); }
__device__ __forceinline__ float fb(u32 v) { return __builtin_bit_cast(float, v); }
__device__ __forceinline__ u32 bf(float v) { return __builtin_bit_cast(u32, v); }
__device__ __forceinline__ int rmap(int m, int hi) { return 4 * (m >> 1) + 2 * hi + (m & 1); }
__device__ __forceinline__ f32x2 fold(f32x4 c) { return (f32x2){c[0], c[1]} + (f32x2){c[2], c[3]}; }
__device__ __forceinline__ u32 pk2(f32x2 f) { return __builtin_bit_cast(u32, __builtin_convertvector(f, bf16x2)); }
__device__ __forceinline__ u32 nos(u32 v) { return v < 0xFFFFFFFEu ? v : 0xFFFFFFFEu; }
__device__ __forceinline__ float hsum(float a, float b) { auto r = __builtin_amdgcn_permlane32_swap(bf(a), bf(b), false, false); return fb(r[0]) + fb(r[1]); }
__device__ __forceinline__ float fmax3(float a, float b, float c) { float r; asm("v_max3_f32 %0, %1, %2, %3" : "=v"(r) : "v"(a), "v"(b), "v"(c)); return r; }
__device__ __forceinline__ float max16(float x) { asm volatile("s_nop 1" : "+v"(x)); auto r = __builtin_amdgcn_permlane16_swap(bf(x), bf(x), false, false); return fmax3(fb(r[0]), fb(r[1]), fb(r[1])); }
__device__ __forceinline__ float max32(float x) { asm volatile("s_nop 1" : "+v"(x)); auto r = __builtin_amdgcn_permlane32_swap(bf(x), bf(x), false, false); return fmax3(fb(r[0]), fb(r[1]), fb(r[1])); }
__device__ __forceinline__ float add16(float x) { auto r = __builtin_amdgcn_permlane16_swap(bf(x), bf(x), false, false); return fb(r[0]) + fb(r[1]); }
__device__ __forceinline__ float add32(float x) { auto r = __builtin_amdgcn_permlane32_swap(bf(x), bf(x), false, false); return fb(r[0]) + fb(r[1]); }
__device__ __forceinline__ float ror8(float x) { return fb(__builtin_amdgcn_update_dpp(0, (int)bf(x), 0x128, 0xf, 0xf, false)); }
__device__ __forceinline__ float mov_hi8(float old, float src) { return fb(__builtin_amdgcn_update_dpp((int)bf(old), (int)bf(src), 0x128, 0xf, 0xc, false)); }

#define BAR() do { asm volatile("s_waitcnt lgkmcnt(0)" ::: "memory"); __builtin_amdgcn_s_barrier(); asm volatile("" ::: "memory"); } while (0)
#define VMCNT(n) asm volatile("s_waitcnt vmcnt(" #n ")" ::: "memory")
#define SMF_A(c, a, w, idx) asm("v_smfmac_f32_16x16x64_bf16 %0, %1, %2, %3" : "+v"(c) : "v"(a), "a"(w), "v"(idx))
#define SMF_V(c, a, w, idx) asm("v_smfmac_f32_16x16x64_bf16 %0, %1, %2, %3" : "+v"(c) : "v"(a), "v"(w), "v"(idx))
#define MF_AV(c, a, b) asm("v_mfma_f32_16x16x32_bf16 %0, %1, %2, %0" : "+v"(c) : "a"(a), "v"(b))
#define MF_VA(c, a, b) asm("v_mfma_f32_16x16x32_bf16 %0, %1, %2, %0" : "+v"(c) : "v"(a), "a"(b))
#define MF_VV(c, a, b) asm("v_mfma_f32_16x16x32_bf16 %0, %1, %2, %0" : "+v"(c) : "v"(a), "v"(b))
#define MF0_AV(c, a, b) asm("v_mfma_f32_16x16x32_bf16 %0, %1, %2, 0" : "=&v"(c) : "a"(a), "v"(b))
#define MF0_VA(c, a, b) asm("v_mfma_f32_16x16x32_bf16 %0, %1, %2, 0" : "=&v"(c) : "v"(a), "a"(b))
#define MF0_VV(c, a, b) asm("v_mfma_f32_16x16x32_bf16 %0, %1, %2, 0" : "=&v"(c) : "v"(a), "v"(b))
#define SB() __builtin_amdgcn_sched_barrier(0)
#define PRE2(a, b) asm volatile("s_nop 1" : "+v"(a), "+v"(b))
#define PRE3(a, b, c) asm volatile("s_nop 1" : "+v"(a), "+v"(b), "+v"(c))
#define PRE4(a, b, c, d) asm volatile("s_nop 1" : "+v"(a), "+v"(b), "+v"(c), "+v"(d))
#define POST2(a, b) asm volatile("s_nop 7" : "+v"(a), "+v"(b))
#define POST3(a, b, c) asm volatile("s_nop 7" : "+v"(a), "+v"(b), "+v"(c))
#define POST4(a, b, c, d) asm volatile("s_nop 7" : "+v"(a), "+v"(b), "+v"(c), "+v"(d))
#define Z4 (f32x4){0.f, 0.f, 0.f, 0.f}

#define STAMP_DECL do {} while (0)
#define STAMP(k) do {} while (0)
#define STAMPW(k) do {} while (0)
#define MARK(k) do {} while (0)
#define STAMP_SET(k, v) do {} while (0)
#define STAMP_FLUSH(s) do {} while (0)
#define STAMP_FLUSH_W(s) do {} while (0)
#define STAMP_FLUSH2(s) do {} while (0)

__device__ __forceinline__ u32 wait_ge(GAS u32* word, u32 want, u32& cap, bool sleep) {
    u32 v, spins = cap;
    for (;;) {
        v = __hip_atomic_load(word, RLX_AGENT);
        if (__builtin_amdgcn_ballot_w64(v < want) == 0 || --spins == 0) break;
        if (sleep) __builtin_amdgcn_s_sleep(4);
    }
    cap = spins ? cap : 1u;
    __builtin_amdgcn_fence(__ATOMIC_ACQUIRE, "wavefront");
    return v;
}
__device__ __forceinline__ u32 mx3(u32 a, u32 b, u32 c) { return max(max(a, b), c); }
__device__ __forceinline__ u32 mx4(u32 t, u32x4 v) { return mx3(mx3(t, v.x, v.y), v.z, v.w); }
__device__ __forceinline__ u64 sel_mask(GAS u32* flags, int lane) {
    const int d = (int)__hip_atomic_load(flags + F_SEL + (lane & 31), RLX_AGENT), ad = d < 0 ? -d : d;
    const bool clear = lane <= NMBLK && ad >= 110 && ad <= 400;
    const u64 cm = __builtin_amdgcn_ballot_w64(clear), up = __builtin_amdgcn_ballot_w64(d > 0);
    const u64 odd = 0xAAAAAAAAAAAAAAAAull;
    const int nc = __builtin_popcountll(cm), na = __builtin_popcountll((up ^ odd) & cm);
    const u64 pat = 2 * na > nc ? ~odd : odd;
    if (4 * max(na, nc - na) >= 3 * nc) return pat;
    return (up & cm) | (pat & ~cm);
}
__device__ __forceinline__ unsigned char* m_base(unsigned char* base, int s, u64 selm) {
    const int b = s / 3;
    return base + OFF_MB + (size_t)b * MB2 + (s - 3 * b) * 655360 + (((selm >> (b + 1)) & 1) << 13);
}
__device__ __forceinline__ u32 wave_min(u32 v) {
    v = min(v, (u32)__builtin_amdgcn_update_dpp(-1, (int)v, 0x111, 0xf, 0xf, false));
    v = min(v, (u32)__builtin_amdgcn_update_dpp(-1, (int)v, 0x112, 0xf, 0xf, false));
    v = min(v, (u32)__builtin_amdgcn_update_dpp(-1, (int)v, 0x114, 0xf, 0xf, false));
    return (u32)__builtin_amdgcn_readlane((int)v, 7);
}

constexpr int N_LV1 = 24;
struct Hs { GAS u64* req; u32x2 want; };
__device__ __forceinline__ void hs_begin(Hs& hs, unsigned char* base) {
    hs.req = (GAS u64*)(base + OFF_HS) + blockIdx.x * 16;
    asm volatile("global_load_dwordx2 %0, %1, off sc1" : "=v"(hs.want) : "v"(hs.req) : "memory");
}
__device__ __forceinline__ void hs_say(const Hs& hs, int word) {
    if (threadIdx.x == 0) __hip_atomic_store(hs.req + word, ((u64)hs.want.y << 32) | hs.want.x, RLX_AGENT);
}
__device__ __forceinline__ void hs_post(Hs& hs) {
    asm volatile("" : "+v"(hs.want));
    const u64 r = (((u64)hs.want.y << 32) | hs.want.x) + 16u;
    hs.want = (u32x2){(u32)r, (u32)(r >> 32)};
    BAR();
    hs_say(hs, 0);
}
__device__ __forceinline__ void hs_peek(const Hs& hs, u32x2& got) { asm volatile("global_load_dwordx2 %0, %1, off sc1" : "=v"(got) : "v"(hs.req + 4096) : "memory"); }
__device__ __forceinline__ bool hs_ok(const Hs& hs, u32x2 got, u32 level) {
    const u64 d = (((u64)got.y << 32) | got.x) - (((u64)hs.want.y << 32) | hs.want.x);
    return __builtin_amdgcn_ballot_w64(d < level || d > 2u) == 0;
}
__device__ __forceinline__ void hs_wait(const Hs& hs, u32 level, u32& cap) {
    for (u32 spins = cap;;) {
        u32x2 got;
        hs_peek(hs, got);
        asm volatile("s_waitcnt vmcnt(0)" : "+v"(got) :: "memory");
        if (hs_ok(hs, got, level)) break;
        if (--spins == 0) { cap = 1u; break; }
    }
}
__device__ __forceinline__ void hs_master(unsigned char* base, LAS unsigned char* L, const int slot, const int job) {
    const int tid = threadIdx.x, w = tid >> 6;
    const rsrc_t RH = mk(base + OFF_HS, 65536);
    volatile LAS u32* st = (volatile LAS u32*)L;
    if (tid < 4) st[tid] = 0u;
    const u32x2 r0 = ld2(RH, slot * 128);
    st4(RH, slot * 128 + 16, 0, (u32x4){~r0.x, ~r0.y, ~r0.x, ~r0.y});
    VMCNT(0);
    BAR();
    u64 last = 0;
    for (u32 spins = 0; spins < SPIN_CAP; ++spins) {
        asm volatile("" ::: "memory");
        const u32x2 a = ld2(RH, slot * 128);
        const u32x4 b = ld4(RH, slot * 128 + 16);
        const bool done = b.x == a.x && b.y == a.y, seen = (b.z == a.x && b.w == a.y) || job == 95;
        const u32 m = (__builtin_amdgcn_ballot_w64(job >= 0 && job < N_LV1 && !done) == 0 ? 1u : 0u) | (__builtin_amdgcn_ballot_w64(job >= N_LV1 && job < 95 && !done) == 0 ? 2u : 0u);
        st[w] = m;
        const u32 all = st[0] & st[1] & st[2] & st[3], lvl = (all & 1u) ? (all == 3u ? 2u : 1u) : 0u;
        const u64 ans = (((u64)a.y << 32) | a.x) + lvl;
        if (spins == 0 || ans != last) st2(RH, 32768 + slot * 128, 0, (u32x2){(u32)ans, (u32)(ans >> 32)});
        last = ans;
        if (lvl == 2u && __builtin_amdgcn_ballot_w64(!seen) == 0) break;
    }
    VMCNT(0);
    BAR();
}
__device__ __forceinline__ float row_sum(float v) {
    v += fb(__builtin_amdgcn_update_dpp(0, (int)bf(v), 0x128, 0xf, 0xf, false)); v += fb(__builtin_amdgcn_update_dpp(0, (int)bf(v), 0x124, 0xf, 0xf, false));
    v += fb(__builtin_amdgcn_update_dpp(0, (int)bf(v), 0x122, 0xf, 0xf, false)); v += fb(__builtin_amdgcn_update_dpp(0, (int)bf(v), 0x121, 0xf, 0xf, false));
    return v;
}
__device__ __forceinline__ void init_ln(unsigned char* base, LAS unsigned char* L, const u32* __restrict__ x, const int seg, Hs& hs) {
    const int tid = threadIdx.x, lane = tid & 63, w = tid >> 6, c = tid & 15, tok = tid >> 4;
    u32x4 v[2][5];
#pragma unroll
    for (int k = 0; k < 2; ++k) {
        const u32x4* row = (const u32x4*)(x + (size_t)(seg * 32 + k * 16 + tok) * (DM / 2));
#pragma unroll
        for (int i = 0; i < 5; ++i) v[k][i] = row[c + 16 * i];
    }
    VMCNT(10);
    hs_post(hs);
    MARK(4);
#pragma unroll 1
    for (int k = 0; k < 2; ++k) {
        f32x2 e[20], s2 = {0.f, 0.f}, q2 = s2;
#pragma unroll
        for (int i = 0; i < 20; ++i) { e[i] = (f32x2){bflo(v[0][i >> 2][i & 3]), bfhi(v[0][i >> 2][i & 3])}; s2 += e[i]; }
#pragma unroll
        for (int i = 0; i < 5; ++i) v[0][i] = v[1][i];
        const float mean = row_sum(s2.x + s2.y) * (1.f / DM);
#pragma unroll
        for (int i = 0; i < 20; ++i) { const f32x2 d = e[i] - mean; q2 += d * d; }
        const float q = row_sum(q2.x + q2.y);
        const float mb = rbf(mean), den = rbf(sqrtf(rbf(rbf(q * (1.f / DM)) + 1e-05f))), rd = 1.f / den;
#pragma unroll
        for (int i = 0; i < 5; ++i) {
            u32x4 o;
#pragma unroll
            for (int j = 0; j < 4; ++j) {
                const u32 db = pk2(e[4 * i + j] - mb);
                const f32x2 d = {bflo(db), bfhi(db)}, t = d * rd;
                o[j] = pk2(__builtin_elementwise_fma(__builtin_elementwise_fma(-t, (f32x2){den, den}, d), (f32x2){rd, rd}, t));
            }
            const int ch = c + 16 * i;
            *(LAS u32x4*)(L + (ch >> 2) * 2048 + k * 1024 + (ch & 3) * 256 + tok * 16) = o;
        }
    }
    BAR();
    MARK(5);
    const rsrc_t RX = mk(base + OFF_XF + (size_t)seg * 40960, 40960);
#pragma unroll
    for (int i = 0; i < 10; ++i) st4(RX, (w * 10 + i) * 1024 + lane * 16, 0, *(LAS u32x4*)(L + (w * 10 + i) * 1024 + lane * 16));
}
__device__ __forceinline__ void init_fill(unsigned char* base, const u32* __restrict__ mem0, const int b, const int cnt) {
    constexpr int N0 = 32768, N1 = N0 + 81920, N2 = N1 + 4096, N3 = N2 + 4096, N4 = N3 + 1024, NF = 72;
    constexpr int PER = 7;
    static_assert(N4 <= NF * PER * 256 && OFF_PM + 64 * 16384 < (1u << 30), "fill");
    const rsrc_t RB = mk(base, 1u << 30);
#pragma unroll 1
    for (int k = 0; k < PER * cnt; ++k) {
        const int i = (b * PER + k) * 256 + threadIdx.x;
        u32x4 v = {SENT, SENT, SENT, SENT};
        int o = OOB;
        if (i < N0) {
            o = i * 16;
            const int lo = ((o >> 14) << 13) | (o & 0x1fff);
            if (lo >= 20480 && lo < 30720) {
                const int cs = (lo - 20480) >> 8, r = (lo >> 4) & 15;
#pragma unroll
                for (int q = 0; q < 4; ++q) v[q] = nos(mem0[((2 * (r >> 2) + (r & 1)) * DM + cs * 16 + 4 * q + (r & 2)) >> 1]);
            }
        } else if (i < N1) o = (int)OFF_MB + (i - N0) * 16;
        else if (i < N2) o = (int)OFF_KM + 63 * 65536 + (i - N1) * 16;
        else if (i < N3) o = (int)OFF_PX + 63 * 65536 + (i - N2) * 16;
        else if (i < N4) o = (int)OFF_PM + 63 * 16384 + (i - N3) * 16;
        st4(RB, o, 0, v);
    }
}
__device__ __forceinline__ void init_cal(unsigned char* base, const int blk) {
    if (__builtin_amdgcn_readfirstlane(threadIdx.x >> 6) != 0) return;
    GAS u32* cal = (GAS u32*)(base + (size_t)blk * MB2 + OFF_CAL);
    __builtin_amdgcn_s_sleep(40);
    u32 m0 = 100000u, m1 = 100000u;
#pragma unroll 1
    for (int i = 0; i < 2 * CAL_N; ++i) {
        u64 t0, t1; u32 v;
        asm volatile("s_memtime %0\n s_waitcnt lgkmcnt(0)\n global_load_dword %2, %3, off sc1\n s_waitcnt vmcnt(0)\n s_memtime %1\n s_waitcnt lgkmcnt(0)"
                     : "=&s"(t0), "=&s"(t1), "=&v"(v) : "v"(cal + (i & 1) * 2048 + (i >> 1) * 64) : "memory");
        const u32 dt = (u32)(t1 - t0);
        if (dt > 310u) { if (i & 1) m1 = min(m1, dt); else m0 = min(m0, dt); }
    }
    if (threadIdx.x == 0) __hip_atomic_store((GAS u32*)(base + OFF_FLAG) + F_SEL + blk, (u32)((int)m0 - (int)m1), RLX_AGENT);
}
__device__ __forceinline__ void init_work(unsigned char* base, LAS unsigned char* L, const u32* __restrict__ x, const u32* __restrict__ mem0, const int job, const u32 level) {
    u32 cap = SPIN_CAP;
    Hs hs;
    if (job < 64) {
        hs_begin(hs, base);
        static_assert(F_WORDS / 4 <= N_LV1 * 256 && F_SEL % 4 == 0, "flags");
        const int f = (job * 256 + (int)threadIdx.x) * 4;
        if (job < N_LV1) st4(mk(base + OFF_FLAG, F_WORDS * 4), f < F_SEL || f >= F_SEL + 32 ? f * 4 : OOB, 0, (u32x4){0u, 0u, 0u, 0u});
        else st1(mk(base + (size_t)min(job - N_LV1, NMBLK) * MB2 + OFF_CAL, 16384), threadIdx.x < 64 ? threadIdx.x * 256 : OOB, 0, 0u);
        init_ln(base, L, x, job, hs);
        if (job >= N_LV1) init_fill(base, mem0, 32 + job - N_LV1, 1);
    } else {
        hs.req = (GAS u64*)(base + OFF_HS) + blockIdx.x * 16;
        const u64 r = __hip_atomic_load(hs.req, RLX_AGENT);
        hs.want = (u32x2){(u32)r, (u32)(r >> 32)};
        hs_post(hs);
        VMCNT(0);
        if (job - 64 <= NMBLK) init_cal(base, job - 64); else init_fill(base, mem0, (job - 65 - NMBLK) * 4, 4);
        MARK(4);
    }
    u32x2 got;
    hs_peek(hs, got);
    asm volatile("s_waitcnt vmcnt(0)" : "+v"(got) :: "memory");
    MARK(6);
    if (!hs_ok(hs, got, 0u)) hs_wait(hs, 0u, cap);
    BAR();
    MARK(7);
    st4(mk((const void*)(hs.req + 2), 16), threadIdx.x == 0 ? 0 : OOB, 0, (u32x4){hs.want.x, hs.want.y, hs.want.x, hs.want.y});
    hs_wait(hs, level, cap);
}

struct Wts { const unsigned char *q, *k, *v, *o, *g, *u, *d; };
constexpr int L_WR = 40960;
__device__ __forceinline__ void dmag(const unsigned char* sbase, u32 vo, u32 dst) {
    asm volatile("s_mov_b32 m0, %2\n\ts_nop 0\n\tglobal_load_lds_dwordx4 %0, %1" :: "v"(vo), "s"(sbase), "s"(dst) : "memory");
}
__device__ __forceinline__ void vm_wait(int n) {
    switch (n) {
#define VW(k) case k: VMCNT(k); break;
        VW(0) VW(4) VW(5) VW(8) VW(10) VW(12) VW(15) VW(16) VW(18) VW(21) VW(22) VW(23) VW(24) VW(25) default: VMCNT(20); break;
    }
}
__device__ __forceinline__ u32x4 frag(LAS unsigned char* p, int second) { const u32x2 a = trd(p), b = trd(p + second); return (u32x4){a.x, a.y, b.x, b.y}; }
template <int NU, int NS, int PACE, int LEVEL, class CNT, class ISSUE, class TAKE, class GO>
__device__ __forceinline__ void ring_load(unsigned char* base, LAS unsigned char* L, int w, u32& cap, Hs& hs, CNT cnt, ISSUE issue, TAKE take, GO go) {
    constexpr int SZ = 30720 / NS / 1024 * 1024, ST = NS > 3 ? 2 : 1;
    const u32 r0 = L_WR + w * 30720;
    u32x2 got[5];
    bool done = false;
    if (LEVEL >= 0) hs_begin(hs, base);
#pragma unroll
    for (int u = 0; u < NS && u < NU; ++u) { issue(u, r0 + u * SZ); if (PACE) __builtin_amdgcn_s_sleep(PACE); }
#pragma unroll
    for (int u = 0; u < NU; ++u) {
        int later = 0;
#pragma unroll
        for (int v = u + 1; v < u + NS && v < NU; ++v) later += cnt(v);
        vm_wait(later);
        take(u, L + r0 + (u % NS) * SZ);
        if (LEVEL >= 0) {
            if (u == 0) hs_post(hs);
#pragma unroll
            for (int k = 0; k < 5; ++k) {
                if (u == (k < 4 ? NU - 2 - (3 - k) * ST : NU - 1)) {
                    asm volatile("" : "+v"(got[k]));
                    if (!done && hs_ok(hs, got[k], LEVEL)) { go(); done = true; }
                }
            }
#pragma unroll
            for (int k = 0; k < 5; ++k)
                if (u == (k < 4 ? NU - 2 - (3 - k) * ST - (NS - 1) : NU - 2)) hs_peek(hs, got[k]);
        }
        if (u + NS < NU) {
            asm volatile("s_waitcnt lgkmcnt(0)" ::: "memory");
            issue(u + NS, r0 + (u % NS) * SZ);
            if (PACE) __builtin_amdgcn_s_sleep(PACE);
        }
    }
    asm volatile("s_waitcnt lgkmcnt(0)" ::: "memory");
    if (LEVEL >= 0) {
        MARK(4);
        if (!done) { hs_wait(hs, LEVEL, cap); MARK(5); go(); }
        hs_say(hs, 3);
    }
}
#define ISSUE8(pa, pb, ld, vo, dst)                                                                                      \
    do {                                                                                                                 \
        _Pragma("unroll") for (int i_ = 0; i_ < 4; ++i_) dmag((pa) + (size_t)i_ * 16 * (ld), vo, (dst) + i_ * 1024);     \
        _Pragma("unroll") for (int i_ = 0; i_ < 4; ++i_) dmag((pb) + (size_t)i_ * 16 * (ld), vo, (dst) + 4096 + i_ * 1024); \
    } while (0)
#define ISSUE8W(pa, ld, vo, dst) do { _Pragma("unroll") for (int i_ = 0; i_ < 8; ++i_) dmag((pa) + (size_t)i_ * 8 * (ld), vo, (dst) + i_ * 1024); } while (0)
#define WO_SETUP()                                                                                                       \
    u32 wvo[5];                                                                                                          \
    _Pragma("unroll") for (int i_ = 0; i_ < 5; ++i_) { const u32 b_ = i_ * 1024 + lane * 16; wvo[i_] = (b_ / 320) * 1280 + b_ % 320; } \
    const unsigned char* const wo_ = W.o + (size_t)h * 128 * 1280 + w * 320
#define ISSUE_WO(es, dst)                                                                                                \
    do { _Pragma("unroll") for (int i_ = 0; i_ < 10; ++i_) dmag(wo_ + ((es) * 32 + (i_ / 5) * 16) * 1280, wvo[i_ % 5], (dst) + i_ * 1024); } while (0)

constexpr int L_MEM = 0, L_IMG = 10240, L_KX = 16384;

__device__ __forceinline__ u32 now_ns() { return (u32)__builtin_amdgcn_s_memrealtime() * 10u; }
__device__ __forceinline__ u32 until(u32 target) {
    u32 t = now_ns();
    if ((int)(target - t) > 20000) return t;
    while ((int)(target - t) >= 40) t = now_ns();
    int r = (int)(target - t);
    if (r <= 0) return t;
    if (r >= 28) asm volatile("s_nop 7\n\ts_nop 5"); else if (r >= 14) asm volatile("s_nop 5");
    return target;
}
__device__ __forceinline__ u32x4 bell_read(rsrc_t rr, int off, int so, int want, u32& cap) {
    u32x4 b;
    u32 spins = cap;
    for (;;) {
        asm volatile("" ::: "memory");
        b = ld4(rr, off, so);
        if ((__builtin_amdgcn_ballot_w64((int)b.x < want) & 15u) == 0) break;
        asm volatile("" : "+s"(spins));
        if (--spins == 0) break;
    }
    cap = spins ? cap : 1u;
    return b;
}
__device__ __forceinline__ u32 latest4(u32 v) {
    const u32 t0 = (u32)__builtin_amdgcn_readlane((int)v, 0), t1 = (u32)__builtin_amdgcn_readlane((int)v, 1);
    const u32 t2 = (u32)__builtin_amdgcn_readlane((int)v, 2), t3 = (u32)__builtin_amdgcn_readlane((int)v, 3);
    const u32 a = (int)(t1 - t0) > 0 ? t1 : t0, c = (int)(t3 - t2) > 0 ? t3 : t2;
    return (int)(c - a) > 0 ? c : a;
}
__device__ __forceinline__ int kstep(int w, int p) { return p < 2 ? w + 4 * p : (p < 4 ? 6 + p : ((w + (p >> 1) - 1) & 3) + 4 * (p & 1)); }
#define GATHER_MEM(RING, ADAPT, OWN, RESET)                                                                                 \
    do {                                                                                                                 \
        int slot_ = slot;                                                                                                \
        asm volatile("" : "+s"(slot_));                                                                                  \
        if (s > 0) {                                                                                                     \
            const u32x4 b_ = bell_read(RR, obell2 + (lane < 4 ? ((s - 1) & 3) * SLOT : 0), sel, s, cap);                \
            ADAPT((__builtin_amdgcn_ballot_w64((int)b_.x == s - 1) & 0xF0u) != 0);                                       \
            tfire = until(latest4(b_.z) + ((RING) ? 0 : T_B));                                                           \
        } else tfire = now_ns();                                                                                         \
        asm volatile("" ::: "memory");                                                                                   \
        g2 = ld4(RR, mo2, slot_); a[0] = ld4(RR, mo0, slot_); a[1] = ld4(RR, mo1, slot_);                                \
        SB();                                                                                                            \
        if (RING) asm volatile("s_memtime %0" : "=s"(cfire) :: "memory");                                                \
        STAMP(6);                                                                                                        \
        SB();                                                                                                            \
        amiss = 0;                                                                                                       \
            \
        MEM_WR(2); u32 t_ = mx4(0u, g2); SB();                                                                           \
        MEM_WR(0); OWN(0, 1); t_ = mx4(t_, a[0]); SB();                                                                  \
        MEM_WR(1); OWN(0, 2); t_ = mx4(t_, a[1]); SB();                                                                  \
        if (__builtin_expect(__builtin_amdgcn_ballot_w64(t_ == SENT) != 0, 0)) {                                         \
            u32 spins = cap;                                                                                             \
            for (;;) {                                                                                                   \
                asm volatile("" ::: "memory");                                                                           \
                g2 = ld4(RR, mo2, slot_); a[0] = ld4(RR, mo0, slot_); a[1] = ld4(RR, mo1, slot_);                        \
                if (__builtin_amdgcn_ballot_w64(mx4(mx4(mx4(0u, g2), a[0]), a[1]) == SENT) == 0) break;                  \
                asm volatile("" : "+s"(spins));                                                                          \
                if (--spins == 0) break;                                                                                 \
            }                                                                                                            \
            cap = spins ? cap : 1u;                                                                                      \
            amiss = 1;                                                                                                   \
            asm volatile("" : "+s"(amiss));                                                                              \
            MEM_WR(2); MEM_WR(0); MEM_WR(1);                                                                             \
            RESET();                                                                                                     \
            OWN(0, 3);                                                                                                   \
        }                                                                                                                \
        STAMP(0);                                                                                                        \
        STAMP_SET(7, amiss);                                                                                             \
        SB();                                                                                                            \
        OWN(1, 3);                                                                                                       \
    } while (0)
#define MEM_WR(k) do { SB(); if ((k) == 0) *(LAS u32x4*)(lw) = a[0]; else if ((k) == 1) *(LAS u32x4*)(lw + 4096) = a[1];   \
                       else if (w < 2) *(LAS u32x4*)(lw + 8192) = g2; SB(); } while (0)
#define MEM_RD(p) do { SB(); a[p] = *(LAS u32x4*)((p) < 4 ? l0 + 6144 + (p) * 1024 : ((p) < 6 ? l1 : ((p) < 8 ? l2 : l3)) + ((p) & 1) * 4096); SB(); } while (0)
#define CHAIN_SETUP()                                                                                                    \
    const int tid = threadIdx.x, lane = tid & 63, w = __builtin_amdgcn_readfirstlane(tid >> 6);                          \
    const int n = lane & 15, g = lane >> 4;                                                                              \
    GAS u32* flags = (GAS u32*)(base + OFF_FLAG);                                                                        \
    const rsrc_t RR = mk(base + OFF_RING, 4 * SLOT);                                                                    \
    const u32 idx = (lane & 2) ? 0xEEEEu : 0x4444u;                                                                      \
    const u32x4 sent4 = {SENT, SENT, SENT, SENT};                                                                        \
    u32 cap = SPIN_CAP
#define SEL_SETUP()                                                                                                      \
    const u64 selm = sel_mask(flags, lane);                                                                              \
    const int sel = (int)(selm & 1) << 13
#define MEM_SETUP()                                                                                                      \
    const int mo0 = xp(20480 + w * 1024 + lane * 16), mo1 = xp(20480 + (w + 4) * 1024 + lane * 16),                      \
              mo2 = w < 2 ? xp(20480 + (w + 8) * 1024 + lane * 16) : OOB;                                                \
    const int obell2 = lane < 8 ? xp(O_BELL + (lane < 4 ? 1024 : 1024) + lane * 128) : OOB;                                                                     \
    LAS unsigned char* const l0 = L + L_MEM + lane * 16;                                                                 \
    LAS unsigned char *lw = l0 + w * 1024, *l1 = l0 + ((w + 1) & 3) * 1024, *l2 = l0 + ((w + 2) & 3) * 1024, *l3 = l0 + ((w + 3) & 3) * 1024; \
    asm volatile("" : "+v"(lw), "+v"(l1), "+v"(l2), "+v"(l3))
#define CLK0() do {} while (0)
#define CLK1() do {} while (0)

__device__ __forceinline__ void role_A(unsigned char* base, LAS unsigned char* L, const Wts& W, const int h) {
    CHAIN_SETUP();
    MEM_SETUP();
    u32x8 wt[40];
    {
        Hs hs;
        __builtin_amdgcn_s_sleep(AB_LATE);
        const u32 vo = (lane >> 3) * 8192 + (lane & 7) * 16, to = (8 * g + (n >> 2)) * 128 + (n & 3) * 8;
        const unsigned char* src = (w < 2 ? W.q : W.k) + h * 256 + (w & 1) * 128;
        ring_load<10, 3, 0, 2>(base, L, w, cap, hs, [](int) { return 8; },
            [&](int u, u32 dst) { ISSUE8W(src + (size_t)kstep(w, u) * 64 * 8192, 8192, vo, dst); },
            [&](int u, LAS unsigned char* p) {
#pragma unroll
                for (int t = 0; t < 4; ++t) { LAS unsigned char* q = p + to + t * 32; wt[u * 4 + t] = cat(frag(q, 512), frag(q + 4096, 512)); } }, [] {});
    }
    SEL_SETUP();
    (void)selm;
    BAR();
    MARK(1);
    u32 knownK = 0;
#define FETCH_KX(s_)                                                                                                     \
    do {                                                                                                                 \
        if (knownK <= (u32)(s_)) knownK = wave_min(wait_ge(flags + F_K + h * 32 + (lane & 1), (u32)(s_) + 1u, cap, false)); \
        const rsrc_t RK = mk(base + OFF_KX + ((size_t)(s_) * NH + h) * 16384 + (w - 2) * 4096, 4096);                    \
        u32x4 k_[4];                                                                                                     \
        _Pragma("unroll") for (int i = 0; i < 4; ++i) k_[i] = ld4(RK, lane * 16 + i * 1024);                             \
        _Pragma("unroll") for (int i = 0; i < 4; ++i) *(LAS u32x4*)(L + L_KX + (w - 2) * 4096 + i * 1024 + lane * 16) = k_[i]; \
    } while (0)
    const float ninf = g < 2 ? -INFINITY : 0.f;
    const f32x4 t0init = {ninf, ninf, ninf, ninf};
    const u32 ipa = L_IMG + (4 * g + (n >> 2)) * 32 + (n & 3) * 8;
    const int pox = xp(h * 512 + (g >> 1) * 256 + rmap(n & 7, g & 1) * 16 + (n >> 3) * 8);
    const int pom = g >= 2 ? xp(16384 + h * 128 + rmap(n & 7, n >> 3) * 8 + (g - 2) * 4) : OOB;
    LAS unsigned char* const iw = L + L_IMG + (64 * (w & 1) + n) * 32 + 16 * (w >> 1) + 4 * g;
    const int pclr = lane < 40 ? xp(lane < 32 ? h * 512 + lane * 16 : 16384 + h * 128 + (lane - 32) * 16) : OOB;
    const int obell = (tid >= 192 && tid < 195 && (h & 7) == 0) ? xp(O_BELL + lane * 512 + (h >> 3) * 128) : OOB;
    const bool ringer = w == 3 && (h & 7) == 0;
    const float cfs = 0.08838834764831845f * 1.4426950408889634f;
    const f32x2 cf2 = {cfs, cfs};
    u32 npc = 28300, tp = T_P + (C_P * npc >> 16), ta = T_A + (C_A * npc >> 16), xp_ = 0, xa_ = 0, run = 0, nc = 0, na = 0, t1 = 0, c1_ = 0, tlast = 0, clast = 0;
    bool pa1 = false;
    CLK0();
#pragma unroll 1
    for (int s = 0; s < NSEG; ++s) {
        const int slot = (s & 3) * SLOT + sel;
        STAMP_DECL;
        u32x4 a[10], kx[8], g2;
        f32x4 c0 = Z4, c1 = Z4, c2 = Z4, c3 = Z4;
        PRE4(c0, c1, c2, c3);
        u32 tfire; u64 cfire, cgot;
        u32 amiss;
        auto adapt = [&](bool cmiss) {
            nc = cmiss ? nc + 2 : (nc ? nc - 1 : 0);
            na = cmiss ? na : (pa1 ? na + 2 : (na ? na - 1 : 0));
            run = cmiss || pa1 ? 0 : run + 1;
            if (nc >= N_UP) { nc = 0; xp_ += T_UP; }
            if (na >= N_UP) { na = 0; xa_ += T_UP; }
            if (run == N_DN) { run = 0; xp_ = xp_ ? xp_ - T_UP : 0; xa_ = xa_ ? xa_ - T_UP : 0; }
            if (s > 2) {
                const float r_ = (float)(tlast - t1) * 65536.f * __builtin_amdgcn_rcpf((float)(clast - c1_));
                npc = (u32)__builtin_amdgcn_readfirstlane((int)fminf(fmaxf(r_, 25000.f), 40000.f));
            }
            tp = T_P + xp_ + (C_P * npc >> 16); ta = T_A + xa_ + (C_A * npc >> 16);
            asm volatile("" : "+s"(tp), "+s"(ta), "+s"(npc));
        };
#define A_MF(c, j, t) do { if ((j) < 8) SMF_A(c, a[j], wt[(j) * 4 + (t)], idx); else SMF_V(c, a[j], wt[(j) * 4 + (t)], idx); SB(); } while (0)
#define A_OWN(j, part) do { if ((part) & 1) A_MF(c0, j, 0); if ((part) & 2) { A_MF(c2, j, 2); A_MF(c1, j, 1); A_MF(c3, j, 3); } } while (0)
#define A_RESET() do { c0 = Z4; c1 = Z4; c2 = Z4; c3 = Z4; PRE4(c0, c1, c2, c3); SB(); } while (0)
        GATHER_MEM(true, adapt, A_OWN, A_RESET);
        BAR();
        asm volatile("s_memtime %0" : "=s"(cgot) :: "memory");
        STAMP(1);
        MEM_RD(2); MEM_RD(3);
        if (ringer) {
            asm volatile("s_waitcnt lgkmcnt(2)" : "+s"(cgot), "+s"(cfire) :: "memory");
            const u32 tq = tfire + (min((u32)cgot - (u32)cfire, 100000u) * npc >> 16) + tp;
            st4(RR, obell, slot, (u32x4){(u32)s + 1u, tq, tq + ta, 0u});
            SB();
        }
#pragma unroll
        for (int j = 2; j < 10; ++j) {
            A_MF(c0, j, 0);
            if (j < 8) MEM_RD(j + 2);
            A_MF(c2, j, 2);
            if (j >= 5 && j < 9) { kx[2 * j - 10] = *(LAS u32x4*)(l0 + L_KX + (2 * j - 10) * 1024); SB(); }
            A_MF(c1, j, 1);
            if (j >= 5 && j < 9) { kx[2 * j - 9] = *(LAS u32x4*)(l0 + L_KX + (2 * j - 9) * 1024); SB(); }
            A_MF(c3, j, 3);
        }
        asm volatile("s_nop 4\n\t"
                     "v_pk_add_f32 v[216:217], %[a0], %[b0]\n\tv_pk_add_f32 v[218:219], %[a2], %[b2]\n\t"
                     "v_cvt_pk_bf16_f32 v216, v216, v217\n\tv_cvt_pk_bf16_f32 v218, v218, v219\n\t"
                     "v_pk_add_f32 v[220:221], %[a1], %[b1]\n\tv_pk_add_f32 v[222:223], %[a3], %[b3]\n\t"
                     "ds_write2st64_b32 %[p], v216, v218 offset1:4\n\t"
                     "v_cvt_pk_bf16_f32 v220, v220, v221\n\tv_cvt_pk_bf16_f32 v222, v222, v223\n\t"
                     "ds_write2st64_b32 %[p], v220, v222 offset0:2 offset1:6"
                     :: [a0] "v"(__builtin_shufflevector(c0, c0, 0, 1)), [b0] "v"(__builtin_shufflevector(c0, c0, 2, 3)),
                        [a1] "v"(__builtin_shufflevector(c1, c1, 0, 1)), [b1] "v"(__builtin_shufflevector(c1, c1, 2, 3)),
                        [a2] "v"(__builtin_shufflevector(c2, c2, 0, 1)), [b2] "v"(__builtin_shufflevector(c2, c2, 2, 3)),
                        [a3] "v"(__builtin_shufflevector(c3, c3, 0, 1)), [b3] "v"(__builtin_shufflevector(c3, c3, 2, 3)), [p] "v"(iw), "s"(cgot)
                     : "memory", "v216", "v217", "v218", "v219", "v220", "v221", "v222", "v223");
        STAMP(2);
        if (s == 0) {
            if (w >= 2) FETCH_KX(0);
            BAR();
#pragma unroll
            for (int i = 0; i < 8; ++i) kx[i] = *(LAS u32x4*)(l0 + L_KX + i * 1024);
        }
        if (w == 0 && s < NSEG - 1) {
            {
                asm volatile(
                    "s_waitcnt lgkmcnt(0)\n\t"
                    "s_barrier\n\t"
                    "ds_read_b64_tr_b16 v[216:217], %[ip]\n\t"
                    "ds_read_b64_tr_b16 v[218:219], %[ip] offset:512\n\t"
                    "ds_read_b64_tr_b16 v[220:221], %[ip] offset:1024\n\t"
                    "ds_read_b64_tr_b16 v[222:223], %[ip] offset:1536\n\t"
                    "ds_read_b64_tr_b16 v[224:225], %[ip] offset:2048\n\t"
                    "ds_read_b64_tr_b16 v[226:227], %[ip] offset:2560\n\t"
                    "ds_read_b64_tr_b16 v[228:229], %[ip] offset:3072\n\t"
                    "ds_read_b64_tr_b16 v[230:231], %[ip] offset:3584\n\t"
                    "s_waitcnt lgkmcnt(6)\n\t"
                    "v_mfma_f32_16x16x32_bf16 v[232:235], %[k0], v[216:219], 0\n\t"
                    "v_mfma_f32_16x16x32_bf16 v[236:239], %[k4], v[216:219], 0\n\t"
                    "s_waitcnt lgkmcnt(4)\n\t"
                    "v_mfma_f32_16x16x32_bf16 v[232:235], %[k1], v[220:223], v[232:235]\n\t"
                    "v_mfma_f32_16x16x32_bf16 v[236:239], %[k5], v[220:223], v[236:239]\n\t"
                    "s_waitcnt lgkmcnt(2)\n\t"
                    "v_mfma_f32_16x16x32_bf16 v[232:235], %[k2], v[224:227], v[232:235]\n\t"
                    "v_mfma_f32_16x16x32_bf16 v[236:239], %[k6], v[224:227], v[236:239]\n\t"
                    "s_waitcnt lgkmcnt(0)\n\t"
                    "v_mfma_f32_16x16x32_bf16 v[232:235], %[k3], v[228:231], v[232:235]\n\t"
                    "v_mfma_f32_16x16x32_bf16 v[236:239], %[k7], v[228:231], v[236:239]\n\t"
                    "v_mfma_f32_16x16x32_bf16 v[240:243], v[216:219], v[216:219], %[ti]\n\t"
                    "v_mfma_f32_16x16x32_bf16 v[240:243], v[220:223], v[220:223], v[240:243]\n\t"
                    "v_mfma_f32_16x16x32_bf16 v[240:243], v[224:227], v[224:227], v[240:243]\n\t"
                    "v_mov_b32_dpp v232, v236 row_ror:8 row_mask:0xf bank_mask:0xc\n\t"
                    "v_mov_b32_dpp v233, v237 row_ror:8 row_mask:0xf bank_mask:0xc\n\t"
                    "v_mfma_f32_16x16x32_bf16 v[240:243], v[228:231], v[228:231], v[240:243]\n\t"
                    "v_mov_b32_dpp v234, v238 row_ror:8 row_mask:0xf bank_mask:0xc\n\t"
                    "v_mov_b32_dpp v235, v239 row_ror:8 row_mask:0xf bank_mask:0xc\n\t"
                    "v_max3_f32 v244, v232, v233, v234\n\t"
                    "v_max_f32 v244, v244, v235\n\t"
                    "s_nop 3\n\t"
                    "v_mov_b32_dpp v240, v242 row_ror:8 row_mask:0xf bank_mask:0xc\n\t"
                    "v_mov_b32_dpp v241, v243 row_ror:8 row_mask:0xf bank_mask:0xc\n\t"
                    "v_max3_f32 v244, v244, v240, v241\n\t"
                    "s_nop 1\n\t"
                    "v_max_f32_dpp v244, v244, v244 row_ror:8 row_mask:0xf bank_mask:0xf\n\t"
                    "v_mov_b32 v245, v244\n\t"
                    "s_nop 1\n\t"
                    "v_permlane16_swap_b32 v244, v245\n\t"
                    "v_max_f32 v244, v244, v245\n\t"
                    "v_mov_b32 v245, v244\n\t"
                    "s_nop 1\n\t"
                    "v_permlane32_swap_b32 v244, v245\n\t"
                    "v_max_f32 v244, v244, v245\n\t"
                    "v_pk_add_f32 v[232:233], v[232:233], v[244:245] op_sel_hi:[1,0] neg_lo:[0,1] neg_hi:[0,1]\n\t"
                    "v_pk_add_f32 v[234:235], v[234:235], v[244:245] op_sel_hi:[1,0] neg_lo:[0,1] neg_hi:[0,1]\n\t"
                    "v_pk_add_f32 v[240:241], v[240:241], v[244:245] op_sel_hi:[1,0] neg_lo:[0,1] neg_hi:[0,1]\n\t"
                    "v_pk_mul_f32 v[232:233], v[232:233], %[cf]\n\t"
                    "v_pk_mul_f32 v[234:235], v[234:235], %[cf]\n\t"
                    "v_pk_mul_f32 v[240:241], v[240:241], %[cf]\n\t"
                    "v_exp_f32 v232, v232\n\t"
                    "v_exp_f32 v233, v233\n\t"
                    "v_exp_f32 v234, v234\n\t"
                    "v_exp_f32 v235, v235\n\t"
                    "v_exp_f32 v240, v240\n\t"
                    "v_exp_f32 v241, v241\n\t"
                    "v_pk_add_f32 v[244:245], v[232:233], v[234:235]\n\t"
                    "v_pk_add_f32 v[244:245], v[244:245], v[240:241]\n\t"
                    "v_add_f32 v244, v244, v245\n\t"
                    "s_nop 1\n\t"
                    "v_add_f32_dpp v244, v244, v244 row_ror:8 row_mask:0xf bank_mask:0xf\n\t"
                    "v_mov_b32 v245, v244\n\t"
                    "s_nop 1\n\t"
                    "v_permlane16_swap_b32 v244, v245\n\t"
                    "v_add_f32 v244, v244, v245\n\t"
                    "v_mov_b32 v245, v244\n\t"
                    "s_nop 1\n\t"
                    "v_permlane32_swap_b32 v244, v245\n\t"
                    "v_add_f32 v244, v244, v245\n\t"
                    "v_rcp_f32 v244, v244\n\t"
                    "s_nop 0\n\t"
                    "v_pk_mul_f32 v[232:233], v[232:233], v[244:245] op_sel_hi:[1,0]\n\t"
                    "v_pk_mul_f32 v[234:235], v[234:235], v[244:245] op_sel_hi:[1,0]\n\t"
                    "v_pk_mul_f32 v[240:241], v[240:241], v[244:245] op_sel_hi:[1,0]\n\t"
                    "v_cvt_pk_bf16_f32 v232, v232, v233\n\t"
                    "v_cvt_pk_bf16_f32 v233, v234, v235\n\t"
                    "buffer_store_dwordx2 v[232:233], %[px], %[rr], %[so] offen sc1\n\t"
                    "v_cvt_pk_bf16_f32 v240, v240, v241\n\t"
                    "buffer_store_dwordx2 v[232:233], %[px], %[rr], %[s2] offen sc1\n\t"
                    "buffer_store_dword v240, %[pm], %[rr], %[so] offen sc1\n\t"
                    "buffer_store_dword v240, %[pm], %[rr], %[s2] offen sc1\n\t"
                    :
                    : [ip] "v"(ipa), [ti] "v"(t0init), [k0] "v"(kx[0]), [k1] "v"(kx[1]), [k2] "v"(kx[2]), [k3] "v"(kx[3]), [k4] "v"(kx[4]),
                      [k5] "v"(kx[5]), [k6] "v"(kx[6]), [k7] "v"(kx[7]), [cf] "v"(cf2), [px] "v"(pox), [pm] "v"(pom), [rr] "s"(RR), [so] "s"(slot), [s2] "s"(slot + 65536)
                    : "memory", "v216", "v217", "v218", "v219", "v220", "v221", "v222", "v223", "v224", "v225", "v226", "v227", "v228", "v229", "v230",
                      "v231", "v232", "v233", "v234", "v235", "v236", "v237", "v238", "v239", "v240", "v241", "v242", "v243", "v244", "v245");
                STAMPW(4);
                st4(RR, pclr, ((s + 2) & 3) * SLOT + sel, sent4);
                st4(RR, pclr, ((s + 2) & 3) * SLOT + sel + 65536, sent4);
            }
        } else if (w < 2) {
            BAR();
            if (s < NSEG - 1) asm volatile("s_nop 7\n\ts_nop 7\n\ts_nop 7\n\ts_nop 7" ::: "memory");
            if (w == 1) {
            const rsrc_t RKM = mk(base + OFF_KM + ((size_t)s * NH + h) * 2048, 2048);
#pragma unroll
            for (int e = 0; e < 4; ++e) {
                const u32x2 lo = trd(L + ipa + e * 1024), hi = trd(L + ipa + e * 1024 + 512);
                st4(RKM, n >= 8 ? e * 512 + (g * 8 + n - 8) * 16 : OOB, 0, (u32x4){lo.x, lo.y, hi.x, hi.y});
            }
            VMCNT(0);
            if (lane == 0) __hip_atomic_store(flags + F_KM + h * 32, (u32)s + 1u, RLX_AGENT);
            }
        } else {
            BAR();
            if (s + 2 < NSEG) FETCH_KX(s + 1);
            st4(RR, obell, ((s + 2) & 3) * SLOT + sel, sent4);
        }
        SB();
        pa1 = amiss != 0 && s > 0;
        tlast = tfire; clast = (u32)cfire;
        if (s == 1) { t1 = tfire; c1_ = (u32)cfire; }
        STAMP_FLUSH(s);
    }
    CLK1();
    if (cap == 1u && lane == 0) __hip_atomic_store(flags + F_TMO, 1u, RLX_AGENT);
}

__device__ __forceinline__ void role_B(unsigned char* base, LAS unsigned char* L, const Wts& W, const int h) {
    CHAIN_SETUP();
    MEM_SETUP();
    u32x8 wt[40];
    {
        Hs hs;
        __builtin_amdgcn_s_sleep(AB_LATE);
        const u32 vo = (lane >> 2) * 8192 + (lane & 3) * 16, to = (8 * g + (n >> 2)) * 64 + (n & 3) * 8, tp = (4 * g + (n >> 2)) * 320 + (n & 3) * 8;
        const size_t co = h * 256 + w * 64;
        WO_SETUP();
        u32x4 lo[10];
        ring_load<9, 3, 0, 2>(base, L, w, cap, hs, [](int u) { return u < 5 ? 8 : 10; },
            [&](int u, u32 dst) {
                if (u < 5) ISSUE8(W.v + co + (size_t)kstep(w, 2 * u) * 64 * 8192, W.v + co + (size_t)kstep(w, 2 * u + 1) * 64 * 8192, 8192, vo, dst);
                else ISSUE_WO(u - 5, dst); },
            [&](int u, LAS unsigned char* p) {
                if (u < 5) {
#pragma unroll
                    for (int t = 0; t < 4; ++t) { LAS unsigned char* q = p + to + (t >> 1) * 4096 + (t & 1) * 32; wt[u * 4 + t] = cat(frag(q, 256), frag(q + 2048, 256)); }
                } else {
#pragma unroll
                    for (int i = 0; i < 10; ++i) {
                        const u32x4 f = frag(p + tp + i * 32, 16 * 320);
                        if (((u - 5) & 1) == 0) lo[i] = f; else wt[20 + 2 * i + ((u - 5) >> 1)] = cat(lo[i], f);
                    }
                } }, [] {});
    }
    SEL_SETUP();
    BAR();
    MARK(1);
    LAS unsigned char* const ip = L + L_IMG + (16 * (g >> 1) + 4 * (g & 1) + ((n >> 2) & 1) + 8 * (n >> 3)) * 32 + 8 * (n & 3);
    LAS unsigned char* const iw = L + L_IMG + (16 * w + 2 * (n >> 2) + (n & 1)) * 32 + 8 * g + 2 * (n & 2);
    const int mso = (10 * w + (g & 1)) * 16384 + h * 256 + n * 16 + (g >> 1) * 8;
    const int mcl = (10 * w + g) * 16384 + h * 256 + n * 16, mcl2 = g < 2 ? mcl : OOB;
    CLK0();
#pragma unroll 1
    for (int s = 0; s < NSEG; ++s) {
        const int slot = (s & 3) * SLOT + sel;
        STAMP_DECL;
        const rsrc_t RM = mk(m_base(base, s, selm), 655360);
        u32x4 a[10], g2;
        f32x4 c0 = Z4, c1 = Z4, c[10];
#pragma unroll
        for (int i = 0; i < 10; ++i) c[i] = Z4;
        PRE4(c[0], c[1], c[2], c[3]); PRE4(c[4], c[5], c[6], c[7]); PRE4(c[8], c[9], c0, c1);
        u32 tfire; u64 cfire, cgot;
        u32 amiss;
        auto adapt = [](bool) {};
#define B_M1(j) do { SMF_A(c0, a[j], wt[(j) * 2], idx); SB(); SMF_A(c1, a[j], wt[(j) * 2 + 1], idx); SB(); } while (0)
#define B_OWN(j, part) do { if ((part) & 1) { SMF_A(c0, a[j], wt[(j) * 2], idx); SB(); } if ((part) & 2) { SMF_A(c1, a[j], wt[(j) * 2 + 1], idx); SB(); } } while (0)
#define B_RESET() do { c0 = Z4; c1 = Z4; PRE2(c0, c1); SB(); } while (0)
        GATHER_MEM(false, adapt, B_OWN, B_RESET);
        (void)tfire; (void)amiss; (void)cfire; (void)cgot;
        BAR();
        STAMP(1);
        MEM_RD(2); MEM_RD(3); MEM_RD(4); MEM_RD(5);
#pragma unroll
        for (int j = 2; j < 10; ++j) { B_M1(j); if (j < 6) MEM_RD(j + 4); }
        POST2(c0, c1);
        STAMP(2);
        *(LAS u32*)(iw) = pk2(fold(c0));
        *(LAS u32*)(iw + 256) = pk2(fold(c1));
        BAR();
        STAMP(3);
        u32x4 av[2];
#pragma unroll
        for (int t = 0; t < 2; ++t) { const u32x2 lo = trd(ip + t * 1024), hi = trd(ip + t * 1024 + 64); av[t] = (u32x4){lo.x, lo.y, hi.x, hi.y}; }
#define B_MF(i, t) do { if ((i) < 6) SMF_A(c[i], av[t], wt[20 + 2 * (i) + (t)], idx); else SMF_V(c[i], av[t], wt[20 + 2 * (i) + (t)], idx); SB(); } while (0)
#define B_E1(i) do { asm volatile("v_pk_add_f32 %0, %2, %3\n\tv_pk_add_f32 %1, %4, %5" : "=&v"(f0), "=&v"(f1) : "v"(__builtin_shufflevector(c[i], c[i], 0, 1)), \
            "v"(__builtin_shufflevector(c[i], c[i], 2, 3)), "v"(__builtin_shufflevector(c[(i) + 1], c[(i) + 1], 0, 1)), "v"(__builtin_shufflevector(c[(i) + 1], c[(i) + 1], 2, 3))); SB(); } while (0)
#define B_E2() do { asm volatile("v_cvt_pk_bf16_f32 v216, %0, %1\n\tv_cvt_pk_bf16_f32 v217, %2, %3" :: "v"(f0.x), "v"(f0.y), "v"(f1.x), "v"(f1.y) : "v216", "v217"); SB(); } while (0)
#define B_E3(i, pad) do { asm volatile(pad "v_permlane16_swap_b32 v216, v217\n\tbuffer_store_dwordx2 v[216:217], %0, %1, %2 offen sc1" :: "v"(mso), "s"(RM), "s"((i) * 16384) : "memory", "v216", "v217"); SB(); } while (0)
        f32x2 f0, f1;
        B_MF(0, 0); B_MF(1, 0); B_MF(0, 1); B_MF(1, 1);
        B_MF(2, 0); B_MF(3, 0); B_MF(2, 1);
#pragma unroll
        for (int i = 2; i < 8; i += 2) {
            B_E1(i - 2); B_MF(i + 1, 1); B_E2(); B_MF(i + 2, 0); B_E3(i - 2, "s_nop 0\n\t"); B_MF(i + 3, 0); B_MF(i + 2, 1);
        }
        B_E1(6); B_MF(9, 1); B_E2(); B_E3(6, "s_nop 1\n\t");
        asm volatile("s_nop 1");
        B_E1(8); B_E2(); B_E3(8, "s_nop 1\n\t");
        STAMPW(4);
        if (s >= M_NOW) {
            VMCNT(0);
            BAR();
            if (tid == 0) __hip_atomic_store(flags + F_M + s * 128 + (h >> 3) * 32 + (h & 7), 1u, RLX_AGENT);
        }
        if (s > 0 && s <= M_NOW && tid == 0) __hip_atomic_store(flags + F_M + (s - 1) * 128 + (h >> 3) * 32 + (h & 7), 1u, RLX_AGENT);
        if (s + 2 < NSEG) {
            const rsrc_t RM2 = mk(m_base(base, s + 2, selm), 655360);
#pragma unroll
            for (int i = 0; i < 3; ++i) st4(RM2, i < 2 ? mcl : mcl2, i * 65536, sent4);
        }
        STAMP_FLUSH(s);
    }
    CLK1();
    if (cap == 1u && lane == 0) __hip_atomic_store(flags + F_TMO, 1u, RLX_AGENT);
}

__device__ __forceinline__ void role_C(unsigned char* base, LAS unsigned char* L, const int cs) {
    CHAIN_SETUP();
    SEL_SETUP();
    const int mo = cs * 16384 + w * 2048 + lane * 16, po = (P_REP && cs >= 20 ? 65536 : 0) + xp(w * 4096 + lane * 16),
              pmo = (P_REP && cs >= 20 ? 65536 : 0) + xp(16384 + (8 * w + 2 * g) * 128 + n * 8);
    LAS float* red = (LAS float*)L + w * 128 + 2 * g * 16 + n;
    LAS unsigned char* rp = L + ((2 * g + ((lane >> 2) & 1)) * 16 + 4 * (lane & 3) + ((lane >> 2) & 2)) * 4;
    const int opub = xp(20480 + cs * 256 + lane * 4), oclr = lane < 16 ? xp(20480 + cs * 256 + lane * 16) : OOB;
    const int obell = lane < 4 ? xp(O_BELL + (cs / 20) * 512 + lane * 128) : OOB, omiss = (lane == 0 && cs % 10 == 0) ? xp(O_BELL + 1536 + (cs / 10) * 128) : OOB;
    u32 tnext = 0;
    if (tid == 0) *(volatile LAS u32*)(L + 2048) = 0u;
    BAR();
    u32 knownU = 0, knownU1 = 0;
#pragma unroll 1
    for (int s = 0; s < NSEG - 1; ++s) {
        const int slot = (s & 3) * SLOT + sel;
        STAMP_DECL;
        if (s < D_TWIN && (s & 1)) { if (knownU1 <= (u32)s) knownU1 = wave_min(wait_ge(flags + F_U + (32 + 8 * w + (lane & 7)) * 32 + cs / 10, (u32)s + 1u, cap, false)); }
        else if (knownU <= (u32)s) knownU = wave_min(wait_ge(flags + F_U + (8 * w + (lane & 7)) * 32 + cs / 10, (u32)s + 1u, cap, false));
        u32x8 uf[4];
        const rsrc_t RU = mk(base + OFF_U + (size_t)s * 1310720 + (size_t)cs * 32768 + w * 8192, 8192);
#pragma unroll
        for (int t = 0; t < U_EARLY; ++t) uf[t] = cat(ld4(RU, lane * 16 + t * 2048), ld4(RU, lane * 16 + t * 2048 + 1024));
        rsrc_t RM = mk(m_base(base, s, selm), 655360);
        int slot_ = slot;
        asm volatile("" : "+s"(RM), "+s"(slot_));
        f32x4 c0 = Z4, c1 = Z4;
        PRE2(c0, c1);
        u32x4 m0, m1, p0, p1, p2, p3, pm;
        if (s > 0) until(tnext + T_BELL);
        const u32x4 bl = bell_read(RR, obell, slot_, s + 1, cap);
        const u32 tref = latest4(bl.y);
        tnext = latest4(bl.z);
        STAMP(4);
#pragma unroll
        for (int t = U_EARLY; t < 4; ++t) uf[t] = cat(ld4(RU, lane * 16 + t * 2048), ld4(RU, lane * 16 + t * 2048 + 1024));
        SB();
        u32 spins = cap;
        until(tref - T_M);
        asm volatile("" ::: "memory");
        m0 = ld4(RM, mo); m1 = ld4(RM, mo + 1024);
        SB();
        until(tref);
        asm volatile("" ::: "memory");
        SB();
        STAMP(5);
#define LD_PM() do { const u32x2 a_ = ld2(RR, pmo, slot_), b_ = ld2(RR, pmo + 128, slot_); pm = (u32x4){a_.x, a_.y, b_.x, b_.y}; } while (0)
        p0 = ld4(RR, po, slot_); SB(); p1 = ld4(RR, po + 1024, slot_); SB(); p2 = ld4(RR, po + 2048, slot_); SB(); p3 = ld4(RR, po + 3072, slot_); SB();
        LD_PM(); SB();
        u32 tm = mx4(mx4(0u, m0), m1);
        SB();
        SMF_V(c0, p0, uf[0], idx); u32 tp = mx4(tm, p0); SB();
        SMF_V(c1, p1, uf[1], idx); tp = mx4(tp, p1); SB();
        SMF_V(c0, p2, uf[2], idx); tp = mx4(tp, p2); SB();
        SMF_V(c1, p3, uf[3], idx); tp = mx4(tp, p3); SB();
        SMF_V(c0, pm, cat(m0, m1), idx); tp = mx4(tp, pm); SB();
        if (__builtin_expect(__builtin_amdgcn_ballot_w64(tp == SENT) != 0, 0)) {
            while (__builtin_amdgcn_ballot_w64(tm == SENT) != 0 && --spins != 0) {
                asm volatile("" ::: "memory");
                m0 = ld4(RM, mo); m1 = ld4(RM, mo + 1024);
                tm = mx4(mx4(0u, m0), m1);
            }
            spins = spins ? spins : 1u;
            for (;;) {
                asm volatile("" ::: "memory");
                p0 = ld4(RR, po, slot_); p1 = ld4(RR, po + 1024, slot_); p2 = ld4(RR, po + 2048, slot_); p3 = ld4(RR, po + 3072, slot_); LD_PM();
                if (__builtin_amdgcn_ballot_w64(mx4(mx4(mx4(mx4(mx4(0u, p0), p1), p2), p3), pm) == SENT) == 0) break;
                asm volatile("" : "+s"(spins));
                if (--spins == 0) break;
            }
            *(volatile LAS u32*)(L + 2048) = (u32)s + 1u;
            c0 = Z4; c1 = Z4;
            PRE2(c0, c1);
            SMF_V(c0, p0, uf[0], idx); SMF_V(c1, p1, uf[1], idx); SMF_V(c0, p2, uf[2], idx); SMF_V(c1, p3, uf[3], idx); SMF_V(c0, pm, cat(m0, m1), idx);
            SB();
            asm volatile("s_nop 3");
        }
        STAMP(6);
        asm volatile("s_nop 5" : "+v"(c0), "+v"(c1));
        const f32x2 f = fold(c0) + fold(c1);
        red[0] = f.x;
        red[16] = f.y;
        int onext = ((s + 1) & 3) * SLOT + sel;
        asm volatile("" : "+s"(onext));
        if (w != 0) BAR();
        else {
            BAR();
            const f32x2 r0 = *(LAS f32x2*)rp, r1 = *(LAS f32x2*)(rp + 512), r2 = *(LAS f32x2*)(rp + 1024), r3 = *(LAS f32x2*)(rp + 1536);
            const f32x2 r = (r0 + r1) + (r2 + r3);
            st1(RR, opub, onext, pk2(r));
            STAMPW(7);
            st4(RR, oclr, ((s + 3) & 3) * SLOT + sel, sent4);
            if ((u32)__builtin_amdgcn_readfirstlane((int)*(volatile LAS u32*)(L + 2048)) == (u32)s + 1u) { st1(RR, omiss, sel, (u32)s + 1u); STAMP_SET(3, 1); }
        }
        BAR();
        cap = spins ? cap : 1u;
        STAMP_FLUSH(s);
    }
    if (cap == 1u && lane == 0) __hip_atomic_store(flags + F_TMO, 1u, RLX_AGENT);
}

constexpr float MASKED = -1.0e9f / 0.08838834764831845f;
constexpr int NTG = 2;
#define G_PACE 6
#define TOUCH_U 0
#define TOUCH_K 0
#define E_NOW 2
#define D_FLAG 14
#define D_LATE 48
#define F_CUT1 36
#define F_CUT2 54
#define S_SPLIT 46
constexpr int L_XF = 0, L_EX = 81920;

template <int I> __device__ __forceinline__ void dma1(const unsigned char* sbase, u32 vo, u32 dst) {
    if constexpr ((I & 3) == 0)
        asm volatile("s_mov_b32 m0, %2\n\ts_nop 0\n\tglobal_load_lds_dwordx4 %0, %1 sc1" :: "v"(vo), "s"(sbase + I * 1024), "s"(dst + I * 1024) : "memory");
    else
        asm volatile("global_load_lds_dwordx4 %0, %1 offset:%2 sc1" :: "v"(vo), "s"(sbase + (I & ~3) * 1024), "i"((I & 3) * 1024) : "memory");
}
__device__ __forceinline__ void dma_i(int i, const unsigned char* sbase, u32 vo, u32 dst) {
    switch (i) {
        case 0: dma1<0>(sbase, vo, dst); break; case 1: dma1<1>(sbase, vo, dst); break; case 2: dma1<2>(sbase, vo, dst); break;
        case 3: dma1<3>(sbase, vo, dst); break; case 4: dma1<4>(sbase, vo, dst); break; case 5: dma1<5>(sbase, vo, dst); break;
        case 6: dma1<6>(sbase, vo, dst); break; case 7: dma1<7>(sbase, vo, dst); break; case 8: dma1<8>(sbase, vo, dst); break;
        default: dma1<9>(sbase, vo, dst); break;
    }
}
__device__ __forceinline__ void dma_xf(const unsigned char* base, int seg, int buf, int w, int lane) {
#pragma unroll
    for (int i = 0; i < 10; ++i) dma_i(i, base + OFF_XF + (size_t)seg * 40960 + w * 10240, lane * 16, L_XF + buf * 40960 + w * 10240);
}
__device__ __forceinline__ void touch(const unsigned char* sbase, u32 vo, int w) {
    asm volatile("s_mov_b32 m0, %2\n\ts_nop 0\n\tglobal_load_lds_dword %0, %1 sc1" :: "v"(vo), "s"(sbase), "s"(98304 + w * 256) : "memory");
}
#define PROJ(NT, acc, WT, O, xb, nsrc, ndst, HOOK) PROJ2(NT, acc, WT, O, xb, if (ks < 10) dma_i(ks, (nsrc), lane * 16, (ndst)); if (ks == 6) { HOOK; })
#define PROJ2(NT, acc, WT, O, xb, HOOK)                                                                                     \
    do {                                                                                                                 \
        constexpr int PD = (NT) == 2 ? 3 : 2;                                                                            \
        u32x4 bq[PD + 1][2];                                                                                             \
        _Pragma("unroll") for (int i_ = 0; i_ < PD; ++i_) {                                                              \
            bq[i_][0] = *(LAS u32x4*)((xb) + i_ * 2048); bq[i_][1] = *(LAS u32x4*)((xb) + i_ * 2048 + 1024);             \
            SB();                                                                                                        \
        }                                                                                                                \
        _Pragma("unroll") for (int ks = 0; ks < 20; ++ks) {                                                              \
            if (ks + PD < 20) {                                                                                          \
                bq[(ks + PD) % (PD + 1)][0] = *(LAS u32x4*)((xb) + (ks + PD) * 2048);                                    \
                bq[(ks + PD) % (PD + 1)][1] = *(LAS u32x4*)((xb) + (ks + PD) * 2048 + 1024);                             \
            }                                                                                                            \
            SB();                                                                                                        \
            _Pragma("unroll") for (int tt = 0; tt < 2; ++tt) _Pragma("unroll") for (int t = 0; t < (NT); ++t) {          \
                if (ks == 0) {                                                                                           \
                    if ((O) + t < 64) MF0_AV(acc[tt * (NT) + t], WT[(O) + t], bq[0][tt]);                                \
                    else MF0_VV(acc[tt * (NT) + t], WT[(O) + t], bq[0][tt]);                                             \
                } else if ((O) + (NT) * ks + t < 64) MF_AV(acc[tt * (NT) + t], WT[(O) + (NT) * ks + t], bq[ks % (PD + 1)][tt]); \
                else MF_VV(acc[tt * (NT) + t], WT[(O) + (NT) * ks + t], bq[ks % (PD + 1)][tt]);                          \
            }                                                                                                            \
            SB();                                                                                                        \
            HOOK;                                                                                                        \
            SB();                                                                                                        \
        }                                                                                                                \
    } while (0)
__device__ __forceinline__ u32x4 pk8(f32x4 a, f32x4 b) { return (u32x4){pk(a[0], a[1]), pk(a[2], a[3]), pk(b[0], b[1]), pk(b[2], b[3])}; }
__device__ __forceinline__ u32x4 keyfrag(u32 a01, u32 a23, u32 b01, u32 b23) {
    auto x = __builtin_amdgcn_permlane16_swap(a01, b01, false, false);
    auto y = __builtin_amdgcn_permlane16_swap(a23, b23, false, false);
    return (u32x4){x[0], x[1], y[0], y[1]};
}
#define LOAD_2MAT(pa, pb, ld)                                                                                            \
    do {                                                                                                                 \
        const u32 vo = (lane >> 2) * (ld) + (lane & 3) * 16, to = (8 * (lane >> 4) + ((lane >> 2) & 3)) * 64 + (lane & 3) * 8; \
        const unsigned char *pa_ = (pa), *pb_ = (pb);                                                                    \
        u32 cap_ = 1; Hs hs_;                                                                                            \
        ring_load<10, 3, G_PACE, -1>(base, L, w, cap_, hs_, [](int) { return 8; },                                               \
            [&](int u, u32 dst) { ISSUE8(pa_ + (size_t)u * 64 * (ld), pb_ + (size_t)u * 64 * (ld), ld, vo, dst); },      \
            [&](int u, LAS unsigned char* p) {                                                                           \
                _Pragma("unroll") for (int t = 0; t < 8; ++t)                                                            \
                    wt[u * 8 + t] = frag(p + to + (t >> 2) * 2048 + ((t >> 1) & 1) * 4096 + (t & 1) * 32, 256); }, [] {}); \
    } while (0)
#define STREAM_SETUP()                                                                                                   \
    const int tid = threadIdx.x, lane = tid & 63, w = __builtin_amdgcn_readfirstlane(tid >> 6);                          \
    GAS u32* flags = (GAS u32*)(base + OFF_FLAG);                                                                        \
    const unsigned char* const xsrc = base + OFF_XF + w * 10240

__device__ __forceinline__ void role_D(unsigned char* base, LAS unsigned char* L, const Wts& W, const int h, const int par) {
    STREAM_SETUP();
    u32 cap = SPIN_CAP;
    Hs hs;
    if (par) __builtin_amdgcn_s_sleep(D_LATE);
    u32x4 wt[80];
    {
        const int n = lane & 15, g = lane >> 4;
        const u32 vo = (lane >> 2) * 8192 + (lane & 3) * 16, to = (8 * g + (n >> 2)) * 64 + (n & 3) * 8, tp = (4 * g + (n >> 2)) * 320 + (n & 3) * 8;
        const size_t co = h * 256 + w * 64;
        WO_SETUP();
        u32x2 lo[10];
        ring_load<18, 6, 0, 1>(base, L, w, cap, hs, [](int u) { return u < 10 ? 4 : 5; },
            [&](int u, u32 dst) {
                if (u < 10) {
#pragma unroll
                    for (int i = 0; i < 4; ++i) dmag(W.v + co + ((size_t)u * 64 + i * 16) * 8192, vo, dst + i * 1024);
                } else {
#pragma unroll
                    for (int i = 0; i < 5; ++i) dmag(wo_ + (u - 10) * 16 * 1280, wvo[i], dst + i * 1024);
                } },
            [&](int u, LAS unsigned char* p) {
                if (u < 10) {
#pragma unroll
                    for (int t = 0; t < 4; ++t) wt[u * 4 + t] = frag(p + to + (t >> 1) * 2048 + (t & 1) * 32, 256);
                } else {
#pragma unroll
                    for (int i = 0; i < 10; ++i) {
                        const u32x2 f = trd(p + tp + i * 32);
                        if (((u - 10) & 1) == 0) lo[i] = f; else wt[40 + 4 * i + ((u - 10) >> 1)] = (u32x4){lo[i].x, lo[i].y, f.x, f.y};
                    }
                } },
            [&] { dma_xf(base, par, 0, w, lane); });
    }
    VMCNT(0);
    BAR();
    MARK(1);
    const rsrc_t RF = mk((const void*)(flags + F_U + (32 * par + h) * 32), 16);
    const int send = par ? D_TWIN : NSEG;
    auto next = [](int s_) { return s_ + (s_ + 2 <= D_TWIN ? 2 : 1); };
    u32x4 vf[8];
    f32x4 acc[4];
#define D_WRITE_V(b)                                                                                                     \
    do {                                                                                                                 \
        *(LAS u32x4*)(L + L_EX + (b) * 8192 + w * 1024 + lane * 16) = pk8(acc[0], acc[1]);                               \
        *(LAS u32x4*)(L + L_EX + (b) * 8192 + 4096 + w * 1024 + lane * 16) = pk8(acc[2], acc[3]);                        \
    } while (0)
    PROJ(2, acc, wt, 0, L + L_XF + lane * 16, xsrc + (size_t)(par + 2) * 40960, L_XF + 40960 + w * 10240, (void)0);
    POST4(acc[0], acc[1], acc[2], acc[3]);
    D_WRITE_V(0);
    VMCNT(0);
    BAR();
    int it = 0;
#pragma unroll 1
    for (int s = par; s < send; s = next(s), ++it) {
        STAMP_DECL;
        STAMPW(0);
        if (it == 4) hs_wait(hs, 2u, cap);
#pragma unroll
        for (int i = 0; i < 4; ++i) {
            vf[i] = *(LAS u32x4*)(L + L_EX + (it & 1) * 8192 + i * 1024 + lane * 16); vf[4 + i] = *(LAS u32x4*)(L + L_EX + (it & 1) * 8192 + 4096 + i * 1024 + lane * 16);
            SB();
        }
        const rsrc_t RU = mk(base + OFF_U + (size_t)s * 1310720, 1310720);
        f32x4 d[10][2];
        u32 e0 = 0, e1 = 0, e2 = 0, e3 = 0;
#define D_EPI(i, e)                                                                                                      \
    do {                                                                                                                 \
        if ((e) == 0) { e0 = pk(d[i][0][0], d[i][0][1]); e1 = pk(d[i][0][2], d[i][0][3]); }                              \
        if ((e) == 1) { e2 = pk(d[i][1][0], d[i][1][1]); e3 = pk(d[i][1][2], d[i][1][3]); }                              \
        if ((e) == 2) st4(RU, lane * 16, (10 * w + (i)) * 32768 + h * 1024, keyfrag(e0, e1, e2, e3));                    \
    } while (0)
#pragma unroll
        for (int i = 0; i < 10; ++i) {
#pragma unroll
            for (int e = 0; e < 4; ++e) {
                if (e == 0) {
                    if (40 + 4 * i < 64) { MF0_VA(d[i][0], vf[0], wt[40 + 4 * i]); MF0_VA(d[i][1], vf[4], wt[40 + 4 * i]); }
                    else { MF0_VV(d[i][0], vf[0], wt[40 + 4 * i]); MF0_VV(d[i][1], vf[4], wt[40 + 4 * i]); }
                } else if (40 + 4 * i + e < 64) { MF_VA(d[i][0], vf[e], wt[40 + 4 * i + e]); MF_VA(d[i][1], vf[4 + e], wt[40 + 4 * i + e]); }
                else { MF_VV(d[i][0], vf[e], wt[40 + 4 * i + e]); MF_VV(d[i][1], vf[4 + e], wt[40 + 4 * i + e]); }
                SB();
                if (i >= 2) D_EPI(i - 2, e);
                if (e == 3) dma_i(i, xsrc + (size_t)(next(next(s)) & (NSEG - 1)) * 40960, lane * 16, L_XF + (it & 1) * 40960 + w * 10240);
                SB();
            }
        }
        STAMPW(1);
        if (next(s) < send) {
            const unsigned char* ub = base + OFF_U + (size_t)s * 1310720 + 10 * w * 32768 + h * 1024;
            const u32 tv = (lane >> 3) * 32768 + (lane & 7) * 128;
            PROJ2(2, acc, wt, 0, L + L_XF + ((it + 1) & 1) * 40960 + lane * 16,
                  if (ks < 3) D_EPI(8, ks); else if (ks < 6) D_EPI(9, ks - 3); else if (ks == D_FLAG) { VMCNT(0); st1(RF, lane == 0 ? w * 4 : OOB, 0, (u32)s + 1u); }
                  else if (TOUCH_U && ks == D_FLAG + 1) touch(ub, tv, w); else if (TOUCH_U && ks == D_FLAG + 2) touch(ub + 8 * 32768, tv & 65535u, w););
            POST4(acc[0], acc[1], acc[2], acc[3]);
            STAMPW(2);
            D_WRITE_V((it + 1) & 1);
            STAMPW(3);
            BAR();
        } else {
            asm volatile("s_nop 7");
            SB();
#pragma unroll
            for (int e = 0; e < 3; ++e) D_EPI(8, e);
#pragma unroll
            for (int e = 0; e < 3; ++e) D_EPI(9, e);
            VMCNT(0);
            st1(RF, lane == 0 ? w * 4 : OOB, 0, (u32)s + 1u);
            BAR();
        }
        STAMPW(4);
        STAMP_FLUSH(s);
    }
}

__device__ __forceinline__ void role_E(unsigned char* base, LAS unsigned char* L, const Wts& W, const int h) {
    STREAM_SETUP();
    u32 cap = SPIN_CAP;
    Hs hs;
    u32x4 wt[80];
    {
        const u32 vo = (lane >> 3) * 8192 + (lane & 7) * 16, to = (8 * (lane >> 4) + ((lane >> 2) & 3)) * 128 + (lane & 3) * 8;
        const unsigned char* src = (w < 2 ? W.k : W.q) + h * 256 + (w & 1) * 128;
        ring_load<20, 7, 0, 1>(base, L, w, cap, hs, [](int) { return 4; },
            [&](int u, u32 dst) {
#pragma unroll
                for (int i = 0; i < 4; ++i) dmag(src + ((size_t)u * 32 + i * 8) * 8192, vo, dst + i * 1024); },
            [&](int u, LAS unsigned char* p) {
#pragma unroll
                for (int t = 0; t < 4; ++t) wt[u * 4 + t] = frag(p + to + t * 32, 512); },
            [&] { dma_xf(base, 0, 0, w, lane); });
    }
    const rsrc_t RF = mk((const void*)(flags + F_K + h * 32), 16);
    VMCNT(0);
    BAR();
    MARK(1);
#pragma unroll 1
    for (int s = 0; s < NSEG; ++s) {
        STAMP_DECL;
        STAMPW(0);
        if (s == 20) hs_wait(hs, 2u, cap);
        f32x4 acc[8];
        LAS unsigned char* xb = L + L_XF + (s & 1) * 40960 + lane * 16;
        PROJ2(4, acc, wt, 0, xb,
              if (ks < 10) dma_i(ks, xsrc + (size_t)((s + 1) & (NSEG - 1)) * 40960, lane * 16, L_XF + ((s + 1) & 1) * 40960 + w * 10240);
              if (ks == 6) { VMCNT(7); st1(RF, lane == 0 ? w * 4 : OOB, 0, (u32)s); }
              if (TOUCH_K && ks == 10) touch(base + OFF_KX + ((size_t)(s ? s - 1 : 0) * NH + h) * 16384 + (w >> 1) * 8192 + (w & 1) * 2048, ((lane >> 4) & 1) * 4096 + (lane & 15) * 128, w););
        asm volatile("" : "+v"(acc[0]), "+v"(acc[1]), "+v"(acc[2]), "+v"(acc[3]));
        POST4(acc[4], acc[5], acc[6], acc[7]);
        STAMPW(1);
        const rsrc_t RK = mk(base + OFF_KX + ((size_t)s * NH + h) * 16384, 16384);
        const int ko = (w >> 1) * 8192 + (w & 1) * 2048;
        st4(RK, lane * 16, ko, pk8(acc[0], acc[1]));
        st4(RK, lane * 16, ko + 1024, pk8(acc[2], acc[3]));
        st4(RK, lane * 16, ko + 4096, pk8(acc[4], acc[5]));
        st4(RK, lane * 16, ko + 5120, pk8(acc[6], acc[7]));
        STAMPW(2);
        if (s < E_NOW) { VMCNT(0); st1(RF, lane == 0 ? w * 4 : OOB, 0, (u32)s + 1u); } else if (TOUCH_K) VMCNT(5); else VMCNT(4);
        STAMPW(3);
        BAR();
        STAMPW(4);
        STAMP_FLUSH(s);
    }
    VMCNT(0);
    st1(RF, lane == 0 ? w * 4 : OOB, 0, (u32)NSEG);
}

__device__ __forceinline__ void role_P(unsigned char* base, const int i) {
    const int tid = threadIdx.x, lane = tid & 63, w = __builtin_amdgcn_readfirstlane(tid >> 6);
    const int n = lane & 15, g = lane >> 4, h = i * 2 + (w >> 1);
    GAS u32* flags = (GAS u32*)(base + OFF_FLAG);
    u32 cap = SPIN_CAP, knownK = 0;
    const int kmo = n >= 8 ? (g * 8 + n - 8) * 16 : OOB;
#pragma unroll 1
    for (int s = w & 1; s < NSEG; s += 2) {
        STAMP_DECL;
        STAMPW(0);
        if (knownK <= (u32)s) knownK = wave_min(wait_ge(flags + F_K + h * 32 + (lane & 3), (u32)s + 1u, cap, true));
        u32x4 kx[8], qf[8], km[4];
        {
            const rsrc_t RK = mk(base + OFF_KX + ((size_t)s * NH + h) * 16384, 16384);
#pragma unroll
            for (int e = 0; e < 8; ++e) { kx[e] = ld4(RK, lane * 16, e * 1024); qf[e] = ld4(RK, lane * 16, 8192 + e * 1024); }
        }
        f32x4 t0[2], t1[2], t2[2];
#pragma unroll
        for (int t = 0; t < 2; ++t) { MF0_VV(t1[t], kx[0], qf[4 * t]); MF0_VV(t2[t], kx[4], qf[4 * t]); }
#pragma unroll
        for (int e = 1; e < 4; ++e)
#pragma unroll
            for (int t = 0; t < 2; ++t) { MF_VV(t1[t], kx[e], qf[4 * t + e]); MF_VV(t2[t], kx[4 + e], qf[4 * t + e]); }
        POST4(t1[0], t1[1], t2[0], t2[1]);
        float y[2][12], mx[2];
#pragma unroll
        for (int t = 0; t < 2; ++t) {
#pragma unroll
            for (int r = 0; r < 4; ++r) {
                y[t][r] = 4 * g + r <= 16 * t + n ? t1[t][r] : MASKED;
                y[t][4 + r] = 16 + 4 * g + r <= 16 * t + n ? t2[t][r] : MASKED;
            }
            mx[t] = fmax3(fmax3(y[t][0], y[t][1], y[t][2]), fmax3(y[t][3], y[t][4], y[t][5]), fmax3(y[t][6], y[t][7], y[t][7]));
        }
        STAMPW(1);
        const rsrc_t RKM = mk(base + OFF_KM + ((size_t)s * NH + h) * 2048, 2048);
        wait_ge(flags + F_KM + h * 32, (u32)(s < NSEG - 1 ? s + 1 : s), cap, true);
        for (u32 spins = cap;;) {
            asm volatile("" ::: "memory");
#pragma unroll
            for (int e = 0; e < 4; ++e) km[e] = ld4(RKM, kmo, e * 512);
            if (s < NSEG - 1 || __builtin_amdgcn_ballot_w64(mx4(mx4(mx4(mx4(0u, km[0]), km[1]), km[2]), km[3]) == SENT) == 0) break;
            if (--spins == 0) { cap = 1u; break; }
            __builtin_amdgcn_s_sleep(2);
        }
        STAMPW(2);
        MF0_VV(t0[0], km[0], qf[0]); MF0_VV(t0[1], km[0], qf[4]);
#pragma unroll
        for (int e = 1; e < 4; ++e) { MF_VV(t0[0], km[e], qf[e]); MF_VV(t0[1], km[e], qf[4 + e]); }
        POST2(t0[0], t0[1]);
        const float cf = 0.08838834764831845f * 1.4426950408889634f;
        float sum[2];
#pragma unroll
        for (int t = 0; t < 2; ++t) {
#pragma unroll
            for (int r = 0; r < 4; ++r) y[t][8 + r] = g >= 2 ? t0[t][r] : -INFINITY;
            mx[t] = max32(max16(fmax3(fmax3(mx[t], y[t][8], y[t][9]), y[t][10], y[t][11])));
        }
#pragma unroll
        for (int t = 0; t < 2; ++t) {
            sum[t] = 0.f;
#pragma unroll
            for (int r = 0; r < 12; ++r) { y[t][r] = __builtin_amdgcn_exp2f((y[t][r] - mx[t]) * cf); sum[t] += y[t][r]; }
            sum[t] = add32(add16(sum[t]));
        }
#pragma unroll
        for (int t = 0; t < 2; ++t) {
            const float rs = __builtin_amdgcn_rcpf(sum[t]);
            const rsrc_t RP = mk(base + OFF_PX + (size_t)s * 65536 + t * 32768 + h * 1024, 1024);
            st4(RP, lane * 16, 0, keyfrag(pk(y[t][0] * rs, y[t][1] * rs), pk(y[t][2] * rs, y[t][3] * rs), pk(y[t][4] * rs, y[t][5] * rs), pk(y[t][6] * rs, y[t][7] * rs)));
            const rsrc_t RQ = mk(base + OFF_PM + (size_t)s * 16384 + t * 8192 + h * 256, 256);
            st2(RQ, g >= 2 ? n * 16 + (g - 2) * 8 : OOB, 0, (u32x2){pk(y[t][8] * rs, y[t][9] * rs), pk(y[t][10] * rs, y[t][11] * rs)});
        }
        STAMPW(3);
        VMCNT(0);
        if (lane == 0) __hip_atomic_store(flags + F_PX + s * 128 + (h >> 3) * 32 + (h & 7), 1u, RLX_AGENT);
        STAMPW(4);
        STAMP_FLUSH_W(s);
    }
    if (cap == 1u && lane == 0) __hip_atomic_store(flags + F_TMO, 1u, RLX_AGENT);
}

__device__ __forceinline__ void role_G(unsigned char* base, LAS unsigned char* L, const Wts& W, const int i) {
    STREAM_SETUP();
    const int set = i % 14, tg = i / 14;
    const int fs = 4 * set + w, fsc = fs < 54 ? fs : 53;
    dma_xf(base, (tg + S_SPLIT) & (NSEG - 1), 0, w, lane);
    u32x4 wt[80];
    LOAD_2MAT(W.g + fsc * 64, W.u + fsc * 64, 3456);
    VMCNT(0);
    BAR();
    int it = 0;
#pragma unroll 1
    for (int k = tg; k < NSEG; k += NTG, ++it) {
        const int s = (k + S_SPLIT) & (NSEG - 1);
        f32x4 acc[8];
        LAS unsigned char* xb = L + L_XF + (it & 1) * 40960 + lane * 16;
        PROJ(4, acc, wt, 0, xb, xsrc + (size_t)((s + NTG) & (NSEG - 1)) * 40960, L_XF + ((it + 1) & 1) * 40960 + w * 10240, (void)0);
        asm volatile("" : "+v"(acc[0]), "+v"(acc[1]), "+v"(acc[2]), "+v"(acc[3]));
        POST4(acc[4], acc[5], acc[6], acc[7]);
#pragma unroll
        for (int q = 0; q < 4; ++q) {
            f32x4& ga = acc[(q >> 1) * 4 + (q & 1)];
            const f32x4 z = ga * 0.f;
#pragma unroll
            for (int r = 0; r < 4; ++r) ga[r] = fmax3(ga[r], z[r], z[r]);
            ga *= acc[(q >> 1) * 4 + (q & 1) + 2];
        }
        const rsrc_t RH = mk(base + OFF_HF + (size_t)s * 110592, 110592);
        st4(RH, fs < 54 ? lane * 16 : OOB, fsc * 1024, pk8(acc[0], acc[1]));
        st4(RH, fs < 54 ? lane * 16 : OOB, 55296 + fsc * 1024, pk8(acc[4], acc[5]));
        VMCNT(2);
        BAR();
        if (tid == 0 && it > 0) __hip_atomic_fetch_add(flags + F_H + ((s - NTG) & (NSEG - 1)), 1u, RLX_AGENT);
    }
    VMCNT(0);
    BAR();
    if (tid == 0) __hip_atomic_fetch_add(flags + F_H + ((tg + NTG * (it - 1) + S_SPLIT) & (NSEG - 1)), 1u, RLX_AGENT);
}

__device__ __forceinline__ void role_F(unsigned char* base, LAS unsigned char* L, const Wts& W, const int cg, const int kb, const int ke, const int kd,
                                       const u32* __restrict__ x, u32* __restrict__ out) {
    const int tid = threadIdx.x, lane = tid & 63, w = __builtin_amdgcn_readfirstlane(tid >> 6);
    const int n = lane & 15, g = lane >> 4;
    GAS u32* flags = (GAS u32*)(base + OFF_FLAG);
    u32 cap = SPIN_CAP;
    u32x4 wd[56];
    {
        Hs hs;
        const u32 vo = (lane >> 3) * 1280 + (lane & 7) * 16, tp = (4 * g + (n >> 2)) * 128 + (n & 3) * 8;
        ring_load<7, 3, 8, -1>(base, L, w, cap, hs, [](int) { return 8; },
            [&](int u, u32 dst) {
#pragma unroll
                for (int k = 0; k < 2; ++k) {
                    const int fs = min(14 * w + 2 * u + k, 53);
#pragma unroll
                    for (int q = 0; q < 4; ++q) dmag(W.d + ((size_t)fs * 32 + 8 * q) * 1280 + cg * 128, vo, dst + k * 4096 + q * 1024);
                } },
            [&](int u, LAS unsigned char* p) {
#pragma unroll
                for (int t = 0; t < 8; ++t) wd[u * 8 + t] = frag(p + tp + (t >> 2) * 4096 + (t & 3) * 32, 2048); }, [] {});
        BAR();
    }
    const int ocol = (cg * 4 + w) * 8 + g * 2;
    u64 okH = 0, okP = 0;
    u32x4 hb[28], pa[2];
    u32x2 x0, x1;
#define F_READY(s_)                                                                                                      \
    do {                                                                                                                 \
        if (!((okH >> (s_)) & 1)) { wait_ge(flags + F_H + (s_), 14u, cap, true); okH = __builtin_amdgcn_ballot_w64(__hip_atomic_load(flags + F_H + lane, RLX_AGENT) >= 14u); } \
        __builtin_amdgcn_fence(__ATOMIC_ACQUIRE, "wavefront");                                                           \
    } while (0)
#define F_LOADH(RH_, k) do { const int vo = 14 * w + (k) < 54 ? lane * 16 : OOB;                                         \
        hb[2 * (k)] = ld4(RH_, vo, (14 * w + (k)) * 1024); hb[2 * (k) + 1] = ld4(RH_, vo, 55296 + (14 * w + (k)) * 1024); } while (0)
    {
        const int s = (kb + S_SPLIT) & (NSEG - 1);
        F_READY(s);
        const rsrc_t RH = mk(base + OFF_HF + (size_t)s * 110592, 110592);
#pragma unroll
        for (int k = 0; k < 14; ++k) F_LOADH(RH, k);
    }
#pragma unroll 1
    for (int k0 = kb; k0 < ke; k0 += kd) {
        const int s = (k0 + S_SPLIT) & (NSEG - 1), sn = k0 + kd < ke ? (k0 + kd + S_SPLIT) & (NSEG - 1) : s;
        STAMP_DECL;
        STAMPW(5);
        F_READY(sn);
        if (s < S_SPLIT && !((okP >> s) & 1)) {
            wait_ge(flags + F_PT + s * 32 + cg * 2 + (w >> 1), 1u, cap, true);
            okP = __builtin_amdgcn_ballot_w64(__hip_atomic_load(flags + F_PT + lane * 32 + cg * 2 + (w >> 1), RLX_AGENT) != 0u);
            __builtin_amdgcn_fence(__ATOMIC_ACQUIRE, "wavefront");
        }
        const rsrc_t RT = mk(base + OFF_PT + (size_t)s * 81920 + (cg * 4 + w) * 2048, 2048);
        pa[0] = ld4(RT, s < S_SPLIT ? lane * 16 : OOB); pa[1] = ld4(RT, s < S_SPLIT ? lane * 16 : OOB, 1024);
        x0 = *(const u32x2*)(x + (s * 32 + n) * (DM / 2) + ocol); x1 = *(const u32x2*)(x + (s * 32 + 16 + n) * (DM / 2) + ocol);
        const rsrc_t RN = mk(base + OFF_HF + (size_t)sn * 110592, 110592);
        SB();
        f32x4 acc[8];
#pragma unroll
        for (int k = 0; k < 14; ++k) {
#pragma unroll
            for (int q = 0; q < 8; ++q) { if (k == 0) MF0_AV(acc[q], wd[q >> 1], hb[q & 1]); else MF_AV(acc[q], wd[4 * k + (q >> 1)], hb[2 * k + (q & 1)]); }
            SB();
            F_LOADH(RN, k);
            SB();
        }
        asm volatile("" : "+v"(acc[0]), "+v"(acc[1]), "+v"(acc[2]), "+v"(acc[3]));
        POST4(acc[4], acc[5], acc[6], acc[7]);
#pragma unroll
        for (int q = 0; q < 8; ++q) *(LAS f32x4*)(L + (w * 8 + q) * 1024 + lane * 16) = acc[q];
        f32x4 r0 = (f32x4){bflo(x0.x), bfhi(x0.x), bflo(x0.y), bfhi(x0.y)} + __builtin_bit_cast(f32x4, pa[0]);
        f32x4 r1 = (f32x4){bflo(x1.x), bfhi(x1.x), bflo(x1.y), bfhi(x1.y)} + __builtin_bit_cast(f32x4, pa[1]);
        BAR();
#pragma unroll
        for (int k = 0; k < 4; ++k) { r0 += *(LAS f32x4*)(L + (k * 8 + 2 * w) * 1024 + lane * 16); r1 += *(LAS f32x4*)(L + (k * 8 + 2 * w + 1) * 1024 + lane * 16); }
        if (s < S_SPLIT) {
            st_out(out, (s * 32 + n) * (DM / 2) + ocol, (u32x2){pk(r0[0], r0[1]), pk(r0[2], r0[3])});
            st_out(out, (s * 32 + 16 + n) * (DM / 2) + ocol, (u32x2){pk(r1[0], r1[1]), pk(r1[2], r1[3])});
            BAR();
        } else {
            st4(RT, lane * 16, 0, __builtin_bit_cast(u32x4, r0)); st4(RT, lane * 16, 1024, __builtin_bit_cast(u32x4, r1));
            VMCNT(0);
            BAR();
            if (tid < 2) __hip_atomic_store(flags + F_PT + s * 32 + cg * 2 + tid, 1u, RLX_AGENT);
        }
        STAMPW(6);
        STAMP_FLUSH2(s);
    }
    if (cap == 1u && lane == 0) __hip_atomic_store(flags + F_TMO, 1u, RLX_AGENT);
}

__device__ __forceinline__ void role_O(unsigned char* base, LAS unsigned char* L, const int cg, const int sb, const int se, const int tt, u32* __restrict__ out) {
    const int tid = threadIdx.x, lane = tid & 63, w = __builtin_amdgcn_readfirstlane(tid >> 6);
    const int n = lane & 15, g = lane >> 4;
    GAS u32* flags = (GAS u32*)(base + OFF_FLAG);
    u32 cap = SPIN_CAP;
    const u64 selm = sel_mask(flags, lane);
    const int ocol = (cg * 2 + (w >> 1)) * 8 + g * 2, otok = (w & 1) * 16 + n;
#pragma unroll 1
    for (int s = sb; s < se; s += 2) {
        STAMP_DECL;
        STAMPW(0);
        if (s < 2) wait_ge(flags + F_M + w * 32, 1u, cap, true);
        GAS u32* const fu = flags + F_U + ((s < D_TWIN ? 32 * (s & 1) : 0) + 8 * w + (lane & 7)) * 32 + cg / 5;
        GAS u32* const fm = flags + F_M + s * 128 + w * 32 + (lane & 7);
        GAS u32* const fp = flags + F_PX + s * 128 + w * 32 + (lane & 7);
        GAS u32* const ft = flags + F_PT + s * 32 + cg;
        const u64 lack = __builtin_amdgcn_ballot_w64(__hip_atomic_load(lane < 8 ? fu : (lane < 16 ? fm : (lane < 32 ? fp : ft)), RLX_AGENT) < (lane < 8 ? (u32)s + 1u : 1u));
        __builtin_amdgcn_fence(__ATOMIC_ACQUIRE, "wavefront");
        if (lack & 0xFFu) wait_ge(fu, (u32)s + 1u, cap, true);
        u32x4 uf[16], mf[4];
        {
            const rsrc_t RU = mk(base + OFF_U + (size_t)s * 1310720 + (size_t)cg * 2 * 32768, 2 * 32768);
#pragma unroll
            for (int k = 0; k < 8; ++k)
#pragma unroll
                for (int c = 0; c < 2; ++c) uf[2 * k + c] = ld4(RU, lane * 16, c * 32768 + (8 * w + k) * 1024);
        }
        const rsrc_t RT = mk(base + OFF_PT + (size_t)s * 81920 + (cg * 4 + w) * 1024, 1024);
        if (s >= S_SPLIT && (lack >> 32)) wait_ge(ft, 1u, cap, true);
        f32x4 r = __builtin_bit_cast(f32x4, ld4(RT, s >= S_SPLIT ? lane * 16 : OOB));
        if (lack & 0xFF00u) wait_ge(fm, 1u, cap, true);
        STAMPW(1);
        {
            const rsrc_t RM = mk(m_base(base, s, selm) + (size_t)cg * 2 * 16384, 2 * 16384);
#pragma unroll
            for (int k = 0; k < 2; ++k)
#pragma unroll
                for (int c = 0; c < 2; ++c) mf[2 * k + c] = ld4(RM, lane * 16, c * 16384 + (2 * w + k) * 1024);
        }
        if ((lack & 0xFFFF0000u) && s < NSEG - 1) wait_ge(fp, 1u, cap, false);
        STAMPW(2);
        f32x4 acc[4];
        {
            const rsrc_t RP = mk(base + OFF_PX + (size_t)s * 65536, 65536);
            const rsrc_t RQ = mk(base + OFF_PM + (size_t)s * 16384, 16384);
            u32x4 pf[16], pm[4];
            const int v0 = tt != 1 ? lane * 16 : OOB, v1 = tt != 0 ? lane * 16 : OOB;
            if (s == NSEG - 1) wait_ge(flags + F_KM + (8 * w + (lane & 7)) * 32, (u32)NSEG, cap, false);
            for (u32 spins = cap;;) {
                asm volatile("" ::: "memory");
#pragma unroll
                for (int k = 0; k < 8; ++k) { pf[2 * k] = ld4(RP, v0, (8 * w + k) * 1024); pf[2 * k + 1] = ld4(RP, v1, 32768 + (8 * w + k) * 1024); }
#pragma unroll
                for (int k = 0; k < 2; ++k) { pm[2 * k] = ld4(RQ, v0, (2 * w + k) * 1024); pm[2 * k + 1] = ld4(RQ, v1, 8192 + (2 * w + k) * 1024); }
                if (s < NSEG - 1) break;
                u32 t_ = 0;
#pragma unroll
                for (int k = 0; k < 16; ++k) t_ = mx4(t_, pf[k]);
#pragma unroll
                for (int k = 0; k < 4; ++k) t_ = mx4(t_, pm[k]);
                if (__builtin_amdgcn_ballot_w64(t_ == SENT) == 0) break;
                if (--spins == 0) { cap = 1u; break; }
            }
#pragma unroll
            for (int k = 0; k < 8; ++k)
#pragma unroll
                for (int q = 0; q < 4; ++q) { if (k == 0) MF0_VV(acc[q], uf[q >> 1], pf[q & 1]); else MF_VV(acc[q], uf[2 * k + (q >> 1)], pf[2 * k + (q & 1)]); }
#pragma unroll
            for (int k = 0; k < 2; ++k)
#pragma unroll
                for (int q = 0; q < 4; ++q) MF_VV(acc[q], mf[2 * k + (q >> 1)], pm[2 * k + (q & 1)]);
        }
        POST4(acc[0], acc[1], acc[2], acc[3]);
        STAMPW(3);
#pragma unroll
        for (int q = 0; q < 4; ++q) *(LAS f32x4*)(L + (w * 4 + q) * 1024 + lane * 16) = acc[q];
        BAR();
#pragma unroll
        for (int k = 0; k < 4; ++k) r += *(LAS f32x4*)(L + (k * 4 + w) * 1024 + lane * 16);
        if (s >= S_SPLIT) {
            if ((w & 1) != 1 - tt) st_out(out, (s * 32 + otok) * (DM / 2) + ocol, (u32x2){pk(r[0], r[1]), pk(r[2], r[3])});
            BAR();
        } else {
            st4(RT, lane * 16, 0, __builtin_bit_cast(u32x4, r));
            VMCNT(0);
            BAR();
            if (tid == 0) __hip_atomic_store(flags + F_PT + s * 32 + cg, 1u, RLX_AGENT);
        }
        STAMPW(4);
        STAMP_FLUSH(s);
    }
    if (cap == 1u && lane == 0) __hip_atomic_store(flags + F_TMO, 1u, RLX_AGENT);
}

constexpr int LDS_MAIN = 163840;

__device__ __forceinline__ void role_of(int b, int& role, int& arg) {
    const int xg = b & 7, j = b >> 3, i = j * 4 + xg - 4;
    const int rl = j < 20 ? 2 : (j < 28 ? 5 : 4), al = j < 20 ? j + 10 * xg : (j < 28 ? j - 20 + 4 * xg : j - 4 + 2 * xg);
    const int rh = i < 64 ? 3 : (i < 88 ? 4 : 6), ah = i < 64 ? i : (i < 88 ? i - 64 : i - 88);
    role = xg >= 4 ? rh : ((xg & 1) ? xg >> 1 : rl);
    arg = xg >= 4 ? ah : ((xg & 1) ? j : al);
}
__device__ __forceinline__ bool is(int role, int r) { asm volatile("" : "+s"(role)); return role == r; }
__device__ __forceinline__ int job_of(int role, int arg) {
    return role == 6 ? 24 + arg : (role == 5 ? (arg == 15 ? 95 : arg) : (role == 2 ? (arg < 31 ? 64 + arg : arg - 16) : -1));
}

__global__ void __launch_bounds__(256, 1) k_main(const u32* __restrict__ x, const u32* __restrict__ mem0, u32* __restrict__ out, unsigned char* __restrict__ base, const unsigned char* wq, const unsigned char* wk,
                                                 const unsigned char* wv, const unsigned char* wo, const unsigned char* wg, const unsigned char* wu, const unsigned char* wd) {
    asm volatile("" :: "s"(x), "s"(mem0), "s"(out), "s"(base), "s"(wq), "s"(wk), "s"(wv), "s"(wo), "s"(wg), "s"(wu), "s"(wd));
    const Wts W = {wq, wk, wv, wo, wg, wu, wd};
    extern __shared__ __attribute__((aligned(16))) unsigned char lds[];
    LAS unsigned char* L = (LAS unsigned char*)lds;
    MARK(0);
    int role, arg;
    role_of(blockIdx.x, role, arg);
    if (is(role, 4)) role_E(base, L, W, arg < 24 ? (arg & 3) * 6 + (arg >> 2) : arg);
    else if (is(role, 3)) {
        role_D(base, L, W, (arg & 3) * 8 + ((arg >> 2) & 7), arg >> 5);
        if (arg >= 32 && arg - 32 < 14 * NTG) role_G(base, L, W, arg - 32);
    }
    else if (is(role >> 1, 0)) {
        if (is(role, 0)) role_A(base, L, W, arg); else role_B(base, L, W, arg);
    } else {
        const int job = job_of(role, arg);
        if (job != 95) init_work(base, L, x, mem0, job, 2u);
        else {
            const int t = threadIdx.x;
            int b;
            if (t < 15 || t == 95) { const int a = t < 15 ? t : 15; b = (20 + (a & 7)) * 8 + (a >> 3) * 2; }
            else if (t < 24) b = (t + 16 - 20) * 8 + 2;
            else if (t < 64) b = ((88 + t - 24) >> 2) * 8 + 4 + ((88 + t - 24) & 3);
            else if (t < 95) b = t - 64 < 20 ? (t - 64) * 8 : (t - 84) * 8 + 2;
            else if (t < 160) b = ((t - 96) >> 1) * 8 + 1 + 2 * (t & 1);
            else if (t < 248) b = ((t - 160) >> 2) * 8 + 4 + (t & 3);
            else b = (28 + ((t - 248) & 3)) * 8 + ((t - 248) >> 2) * 2;
            hs_master(base, L, b, t < 96 ? t : -1);
        }
        MARK(1);
        if (role == 2) role_C(base, L, arg);
        else if (role == 5) role_P(base, arg);
    }
    if ((role == 3 && (arg & 31) < 30) || (role == 4 && arg < 20)) {
        const int a = arg & 31, kb = role == 4 ? F_CUT2 : (arg >= 32 ? 0 : F_CUT1), ke = role == 4 ? NSEG : (arg >= 32 ? F_CUT1 : F_CUT2);
        BAR();
        role_F(base, L, W, a % 10, kb + a / 10, ke, role == 4 ? 2 : 3, x, out);
    }
    if (role == 6 || (role == 3 && (arg & 31) < 20)) {
        MARK(2);
        role_O(base, L, role == 6 ? arg % 20 : arg & 31, role == 6 ? arg / 20 : NSEG - 1, role == 6 ? NSEG - 1 : NSEG, role == 6 ? 2 : arg >> 5, out);
    }
    MARK(3);
}

extern "C" void kernel_launch(void* const* d_in, const int* in_sizes, int n_in, void* d_out, int out_size, void* d_ws, size_t ws_size, hipStream_t stream) {
    (void)in_sizes; (void)n_in; (void)out_size; (void)ws_size;
    unsigned char* base = (unsigned char*)(((size_t)d_ws + (2u << 20) - 1) & ~(size_t)((2u << 20) - 1));
    (void)hipFuncSetAttribute((const void*)k_main, hipFuncAttributeMaxDynamicSharedMemorySize, LDS_MAIN);
    k_main<<<256, 256, LDS_MAIN, stream>>>((const u32*)d_in[0], (const u32*)d_in[1], (u32*)d_out, base, (const unsigned char*)d_in[2], (const unsigned char*)d_in[3],
                                           (const unsigned char*)d_in[4], (const unsigned char*)d_in[5], (const unsigned char*)d_in[8], (const unsigned char*)d_in[9],
                                           (const unsigned char*)d_in[10]);
}
```
